```python
import torch

def _softmax(x, axis=-1):
    return torch.softmax(x, dim=axis)
def _gelu(x):
    return torch.nn.functional.gelu(x, approximate="tanh")
def _silu(x):
    return torch.nn.functional.silu(x)
def _sigmoid(x):
    return torch.sigmoid(x)
def _layernorm(x, axis=-1, eps=1e-5):
    m = x.mean(dim=axis, keepdim=True)
    v = x.var(dim=axis, keepdim=True, unbiased=False)
    return (x - m) / torch.sqrt(v + eps)
def _scatter_add(tgt, idx, src, axis=0):
    return tgt.clone().index_add_(axis, idx, src)
def _segment_sum(src, seg, n):
    out = torch.zeros((n,) + src.shape[1:], dtype=src.dtype)
    return out.index_add_(0, seg, src)
def _pad(x, pw):
    flat = tuple(v for pair in reversed(pw) for v in pair)
    return torch.nn.functional.pad(x, flat)

kernel_name = 'standalone__w8a8_smoothquant_per_tensor_per_channel_int8_activations_weights__sampling'

def setup_inputs():

    _w = (lambda a: (lambda t: t.to(torch.bfloat16) if t.is_floating_point() else t)(torch.as_tensor(a)))
    B, S, D, F = 32, 16, 1024, 3584
    x = abs((lambda *s: torch.randn(*s, dtype=torch.bfloat16) * 0.05)(B, S, D))
    w = (lambda *s: torch.randn(*s, dtype=torch.bfloat16) * 0.05)(D, F)
    x_min, x_max = float(x.min()), float(x.max())
    sx = (x_max - x_min) / 255.0 + 1e-6
    zx = int(torch.as_tensor(-128.0 - x_min / sx).round())
    qx = torch.clamp(torch.as_tensor(x / sx).round() + zx, -128, 127).to(torch.int32)
    sw = (torch.abs(w).amax(dim=0) / 127.0 + 1e-6).to(float)
    qw = torch.clamp(torch.as_tensor(w / sw[None, :]).round(), -127, 127).to(torch.int32)
    return {
        "qx": _w(qx),
        "qw": _w(qw),
        "scale_x": _w(torch.tensor(sx, dtype=float)),
        "zero_x": _w(torch.tensor(zx, dtype=torch.int32)),
        "scale_w": _w(sw),
        "bias": _w((lambda *s: torch.randn(*s, dtype=torch.bfloat16) * 0.05)(F)),
    }

def reference(qx, qw, scale_x, zero_x, scale_w, bias):
    dt = scale_w.dtype
    acc = torch.einsum("bsd,df->bsf", (lambda x, dt: x.to(dt))(qx, dt), (lambda x, dt: x.to(dt))(qw, dt))
    w_colsum = torch.sum((lambda x, dt: x.to(dt))(qw, dt), axis=0)
    acc = acc - (lambda x, dt: x.to(dt))(zero_x, dt) * w_colsum[None, None, :]
    return acc * scale_x * scale_w[None, None, :] + bias[None, None, :]
```

```cpp
#include <hip/hip_runtime.h>
#include <hip/hip_bf16.h>
#include <stdint.h>

typedef int v4i __attribute__((ext_vector_type(4)));
typedef unsigned v4u __attribute__((ext_vector_type(4)));
typedef unsigned v2u __attribute__((ext_vector_type(2)));
typedef float v2f __attribute__((ext_vector_type(2)));
typedef __bf16 v2bf __attribute__((ext_vector_type(2)));

constexpr int M_ = 512, K_ = 1024, N_ = 3584;
constexpr int BM = 128, BN = 64, KB = 128, NBLK = K_ / KB;
constexpr int PF = 3;

__device__ __forceinline__ unsigned pack4(unsigned a, unsigned b, unsigned c, unsigned d) {
    unsigned lo = __builtin_amdgcn_perm(b, a, 0x0c0c0400u);
    unsigned hi = __builtin_amdgcn_perm(d, c, 0x04000c0cu);
    return lo | hi;
}
__device__ __forceinline__ v2f rnd2(v2f v) {
    v2bf b = __builtin_convertvector(v, v2bf);
    unsigned u = __builtin_bit_cast(unsigned, b);
    v2f r; r.x = __builtin_bit_cast(float, u << 16); r.y = __builtin_bit_cast(float, u & 0xffff0000u);
    return r;
}
__device__ __forceinline__ float bf2f(unsigned short h) { return __builtin_bit_cast(float, (unsigned)h << 16); }

__global__ __launch_bounds__(512, 2)
void w8a8_kernel(const int* __restrict__ qx, const int* __restrict__ qw,
                 const unsigned short* __restrict__ sxp, const int* __restrict__ zxp,
                 const unsigned short* __restrict__ swp, const unsigned short* __restrict__ biasp,
                 unsigned short* __restrict__ out) {
    __shared__ __attribute__((aligned(16))) unsigned lds[(2 * 8192 + 8 * 2048 + 256) / 4];
    const int tid = threadIdx.x;
    const int lane = tid & 63;
    const int wv = __builtin_amdgcn_readfirstlane(tid >> 6);
    const int bid = blockIdx.x;
    const int xcd = bid & 7, idx = bid >> 3;
    const int strip = xcd * 7 + (idx >> 2), mt = idx & 3;
    const int m0 = mt * BM, n0 = strip * BN;

    auto rx = __builtin_amdgcn_make_buffer_rsrc((void*)qx, 0, M_ * K_ * 4, 0x00020000);
    auto rw = __builtin_amdgcn_make_buffer_rsrc((void*)qw, 0, K_ * N_ * 4, 0x00020000);

    const int r8 = lane >> 3, q8 = lane & 7;
    const int g = lane >> 4, c = lane & 15;
    const unsigned xoff = ((m0 + 16 * wv + r8) * K_ + 4 * q8) * 4;
    const unsigned woff = ((32 * g + 4 * wv) * N_ + n0 + 4 * c) * 4;

    v4u xr[NBLK][8], wr[NBLK][4];
    auto issue = [&](int b) {
#pragma unroll
        for (int j = 0; j < 4; ++j)
            wr[b][j] = __builtin_amdgcn_raw_buffer_load_b128(rw, woff, (128 * b + j) * N_ * 4, 0);
#pragma unroll
        for (int hh = 0; hh < 2; ++hh)
#pragma unroll
            for (int i = 0; i < 4; ++i)
                xr[b][hh * 4 + i] = __builtin_amdgcn_raw_buffer_load_b128(rx, xoff + b * 512 + i * 128, hh * 8 * 4096, 0);
    };
#pragma unroll
    for (int b = 0; b < PF; ++b) issue(b);

    const v2u swv = *(const v2u*)(swp + n0 + 4 * c);
    const v2u biv = *(const v2u*)(biasp + n0 + 4 * c);
    const float sx = bf2f(sxp[0]);
    const int zx = zxp[0];

    unsigned* csum = lds + (2 * 8192 + 8 * 2048) / 4;
    if (tid < 64) csum[tid] = 0;

    char* ldsb = (char*)lds;
    char* xa = ldsb + 2 * 8192 + wv * 2048;
    v4i acc[4];
#pragma unroll
    for (int t = 0; t < 4; ++t) acc[t] = (v4i){0, 0, 0, 0};
    int cs[4] = {0, 0, 0, 0};

#pragma unroll
    for (int b = 0; b < NBLK; ++b) {
        char* wb = ldsb + (b & 1) * 8192;
#pragma unroll
        for (int t = 0; t < 4; ++t) {
            unsigned p = pack4(wr[b][0][t], wr[b][1][t], wr[b][2][t], wr[b][3][t]);
            cs[t] = __builtin_amdgcn_sdot4(p, 0x01010101, cs[t], false);
            *(unsigned*)(wb + (wv * 4 + t) * 256 + c * 16 + g * 4) = p;
        }
#pragma unroll
        for (int hh = 0; hh < 2; ++hh) {
            v4u p;
#pragma unroll
            for (int i = 0; i < 4; ++i) {
                v4u v = xr[b][hh * 4 + i];
                p[i] = pack4(v[0], v[1], v[2], v[3]);
            }
            *(v4u*)(xa + q8 * 256 + (((8 * hh + r8) ^ q8) * 16)) = p;
        }
        if (b + PF < NBLK) issue(b + PF);
        __syncthreads();
#pragma unroll
        for (int s = 0; s < 2; ++s) {
            const int q = 4 * s + g;
            v4i a = *(const v4i*)(xa + q * 256 + ((c ^ q) * 16));
#pragma unroll
            for (int t = 0; t < 4; ++t) {
                v4i bb = *(const v4i*)(wb + q * 1024 + t * 256 + c * 16);
                acc[t] = __builtin_amdgcn_mfma_i32_16x16x64_i8(a, bb, acc[t], 0, 0, 0);
            }
        }
    }
#pragma unroll
    for (int t = 0; t < 4; ++t) atomicAdd(&csum[4 * c + t], (unsigned)cs[t]);
    __syncthreads();
    v4i cst = *(const v4i*)(csum + 4 * c);

    const float zf = rnd2((v2f){(float)zx, 0.f}).x;
    v2f zc[2], sw[2], bi[2];
#pragma unroll
    for (int p = 0; p < 2; ++p) {
        v2f cf = rnd2((v2f){(float)cst[2 * p], (float)cst[2 * p + 1]});
        zc[p] = rnd2(cf * zf);
        sw[p].x = __builtin_bit_cast(float, swv[p] << 16); sw[p].y = __builtin_bit_cast(float, swv[p] & 0xffff0000u);
        bi[p].x = __builtin_bit_cast(float, biv[p] << 16); bi[p].y = __builtin_bit_cast(float, biv[p] & 0xffff0000u);
    }
#pragma unroll
    for (int r = 0; r < 4; ++r) {
        v2u o;
#pragma unroll
        for (int p = 0; p < 2; ++p) {
            v2f v = rnd2((v2f){(float)acc[2 * p][r], (float)acc[2 * p + 1][r]});
            v = rnd2(v - zc[p]);
            v = rnd2(v * sx);
            v = rnd2(v * sw[p]);
            v = v + bi[p];
            o[p] = __builtin_bit_cast(unsigned, __builtin_convertvector(v, v2bf));
        }
        const int row = m0 + 16 * wv + 4 * g + r;
        *(v2u*)(out + row * N_ + n0 + 4 * c) = o;
    }
}

extern "C" void kernel_launch(void* const* d_in, const int* in_sizes, int n_in,
                              void* d_out, int out_size, void* d_ws, size_t ws_size,
                              hipStream_t stream) {
    w8a8_kernel<<<224, 512, 0, stream>>>((const int*)d_in[0], (const int*)d_in[1],
                                          (const unsigned short*)d_in[2], (const int*)d_in[3],
                                          (const unsigned short*)d_in[4], (const unsigned short*)d_in[5],
                                          (unsigned short*)d_out);
}
```

```cpp
#define NTAUX 0
#include <hip/hip_runtime.h>
#include <hip/hip_bf16.h>
#include <stdint.h>

typedef int v4i __attribute__((ext_vector_type(4)));
typedef unsigned v4u __attribute__((ext_vector_type(4)));
typedef unsigned v2u __attribute__((ext_vector_type(2)));
typedef float v2f __attribute__((ext_vector_type(2)));
typedef __bf16 v2bf __attribute__((ext_vector_type(2)));

constexpr int M_ = 512, K_ = 1024, N_ = 3584;
constexpr int BM = 128, BN = 64, KB = 128, NBLK = K_ / KB;
constexpr int PF = 3;

__device__ __forceinline__ unsigned pack4(unsigned a, unsigned b, unsigned c, unsigned d) {
    unsigned lo = __builtin_amdgcn_perm(b, a, 0x0c0c0400u);
    unsigned hi = __builtin_amdgcn_perm(d, c, 0x04000c0cu);
    return lo | hi;
}
__device__ __forceinline__ v2f rnd2(v2f v) {
    v2bf b = __builtin_convertvector(v, v2bf);
    unsigned u = __builtin_bit_cast(unsigned, b);
    v2f r; r.x = __builtin_bit_cast(float, u << 16); r.y = __builtin_bit_cast(float, u & 0xffff0000u);
    return r;
}
__device__ __forceinline__ float bf2f(unsigned short h) { return __builtin_bit_cast(float, (unsigned)h << 16); }

__global__ __launch_bounds__(512, 2)
void w8a8_kernel(const int* __restrict__ qx, const int* __restrict__ qw,
                 const unsigned short* __restrict__ sxp, const int* __restrict__ zxp,
                 const unsigned short* __restrict__ swp, const unsigned short* __restrict__ biasp,
                 unsigned short* __restrict__ out) {
    __shared__ __attribute__((aligned(16))) unsigned lds[(2 * 8192 + 8 * 2048 + 256) / 4];
    const int tid = threadIdx.x;
    const int lane = tid & 63;
    const int wv = __builtin_amdgcn_readfirstlane(tid >> 6);
    const int bid = blockIdx.x;
    const int xcd = bid & 7, idx = bid >> 3;
    const int strip = xcd * 7 + (idx >> 2), mt = idx & 3;
    const int m0 = mt * BM, n0 = strip * BN;

    auto rx = __builtin_amdgcn_make_buffer_rsrc((void*)qx, 0, M_ * K_ * 4, 0x00020000);
    auto rw = __builtin_amdgcn_make_buffer_rsrc((void*)qw, 0, K_ * N_ * 4, 0x00020000);

    const int r8 = lane >> 3, q8 = lane & 7;
    const int g = lane >> 4, c = lane & 15;
    const unsigned xoff = ((m0 + 16 * wv + r8) * K_ + 4 * q8) * 4;
    const unsigned woff = ((32 * g + 4 * wv) * N_ + n0 + 4 * c) * 4;

    v4u xr[NBLK][8], wr[NBLK][4];
    const int rot = 2 * mt + (idx >> 2);
    auto issue = [&](int b) {
        const int bb = (b + rot) & 7;
#pragma unroll
        for (int j = 0; j < 4; ++j) {
            if (b < 2) wr[b][j] = __builtin_amdgcn_raw_buffer_load_b128(rw, woff, (128 * bb + j) * N_ * 4, NTAUX);
            else wr[b][j] = __builtin_amdgcn_raw_buffer_load_b128(rw, woff, (128 * bb + j) * N_ * 4, 0);
        }
#pragma unroll
        for (int hh = 0; hh < 2; ++hh)
#pragma unroll
            for (int i = 0; i < 4; ++i)
                xr[b][hh * 4 + i] = __builtin_amdgcn_raw_buffer_load_b128(rx, xoff + i * 128, hh * 8 * 4096 + bb * 512, 0);
    };
#pragma unroll
    for (int b = 0; b < PF; ++b) issue(b);

    const v2u swv = *(const v2u*)(swp + n0 + 4 * c);
    const v2u biv = *(const v2u*)(biasp + n0 + 4 * c);
    const float sx = bf2f(sxp[0]);
    const int zx = zxp[0];

    unsigned* csum = lds + (2 * 8192 + 8 * 2048) / 4;
    if (tid < 64) csum[tid] = 0;

    char* ldsb = (char*)lds;
    char* xa = ldsb + 2 * 8192 + wv * 2048;
    v4i acc[4];
#pragma unroll
    for (int t = 0; t < 4; ++t) acc[t] = (v4i){0, 0, 0, 0};
    int cs[4] = {0, 0, 0, 0};

#pragma unroll
    for (int b = 0; b < NBLK; ++b) {
        char* wb = ldsb + (b & 1) * 8192;
#pragma unroll
        for (int t = 0; t < 4; ++t) {
            unsigned p = pack4(wr[b][0][t], wr[b][1][t], wr[b][2][t], wr[b][3][t]);
            cs[t] = __builtin_amdgcn_sdot4(p, 0x01010101, cs[t], false);
            *(unsigned*)(wb + (wv * 4 + t) * 256 + c * 16 + g * 4) = p;
        }
#pragma unroll
        for (int hh = 0; hh < 2; ++hh) {
            v4u p;
#pragma unroll
            for (int i = 0; i < 4; ++i) {
                v4u v = xr[b][hh * 4 + i];
                p[i] = pack4(v[0], v[1], v[2], v[3]);
            }
            *(v4u*)(xa + q8 * 256 + (((8 * hh + r8) ^ q8) * 16)) = p;
        }
        if (b + PF < NBLK) issue(b + PF);
        __syncthreads();
#pragma unroll
        for (int s = 0; s < 2; ++s) {
            const int q = 4 * s + g;
            v4i a = *(const v4i*)(xa + q * 256 + ((c ^ q) * 16));
#pragma unroll
            for (int t = 0; t < 4; ++t) {
                v4i bb = *(const v4i*)(wb + q * 1024 + t * 256 + c * 16);
                acc[t] = __builtin_amdgcn_mfma_i32_16x16x64_i8(a, bb, acc[t], 0, 0, 0);
            }
        }
    }
#pragma unroll
    for (int t = 0; t < 4; ++t) atomicAdd(&csum[4 * c + t], (unsigned)cs[t]);
    __syncthreads();
    v4i cst = *(const v4i*)(csum + 4 * c);

    const float zf = rnd2((v2f){(float)zx, 0.f}).x;
    v2f zc[2], sw[2], bi[2];
#pragma unroll
    for (int p = 0; p < 2; ++p) {
        v2f cf = rnd2((v2f){(float)cst[2 * p], (float)cst[2 * p + 1]});
        zc[p] = rnd2(cf * zf);
        sw[p].x = __builtin_bit_cast(float, swv[p] << 16); sw[p].y = __builtin_bit_cast(float, swv[p] & 0xffff0000u);
        bi[p].x = __builtin_bit_cast(float, biv[p] << 16); bi[p].y = __builtin_bit_cast(float, biv[p] & 0xffff0000u);
    }
#pragma unroll
    for (int r = 0; r < 4; ++r) {
        v2u o;
#pragma unroll
        for (int p = 0; p < 2; ++p) {
            v2f v = rnd2((v2f){(float)acc[2 * p][r], (float)acc[2 * p + 1][r]});
            v = rnd2(v - zc[p]);
            v = rnd2(v * sx);
            v = rnd2(v * sw[p]);
            v = v + bi[p];
            o[p] = __builtin_bit_cast(unsigned, __builtin_convertvector(v, v2bf));
        }
        const int row = m0 + 16 * wv + 4 * g + r;
        *(v2u*)(out + row * N_ + n0 + 4 * c) = o;
    }
}

extern "C" void kernel_launch(void* const* d_in, const int* in_sizes, int n_in,
                              void* d_out, int out_size, void* d_ws, size_t ws_size,
                              hipStream_t stream) {
    w8a8_kernel<<<224, 512, 0, stream>>>((const int*)d_in[0], (const int*)d_in[1],
                                          (const unsigned short*)d_in[2], (const int*)d_in[3],
                                          (const unsigned short*)d_in[4], (const unsigned short*)d_in[5],
                                          (unsigned short*)d_out);
}
```

```cpp
#define NTAUX 0
#include <hip/hip_runtime.h>
#include <hip/hip_bf16.h>
#include <stdint.h>

typedef int v4i __attribute__((ext_vector_type(4)));
typedef unsigned v4u __attribute__((ext_vector_type(4)));
typedef unsigned v2u __attribute__((ext_vector_type(2)));
typedef float v2f __attribute__((ext_vector_type(2)));
typedef __bf16 v2bf __attribute__((ext_vector_type(2)));

constexpr int M_ = 512, K_ = 1024, N_ = 3584;
constexpr int BM = 128, BN = 64, KB = 128, NBLK = K_ / KB;
constexpr int PF = 3;

__device__ __forceinline__ unsigned pack4(unsigned a, unsigned b, unsigned c, unsigned d) {
    unsigned lo = __builtin_amdgcn_perm(b, a, 0x0c0c0400u);
    unsigned hi = __builtin_amdgcn_perm(d, c, 0x04000c0cu);
    return lo | hi;
}
__device__ __forceinline__ v2f rnd2(v2f v) {
    v2bf b = __builtin_convertvector(v, v2bf);
    unsigned u = __builtin_bit_cast(unsigned, b);
    v2f r; r.x = __builtin_bit_cast(float, u << 16); r.y = __builtin_bit_cast(float, u & 0xffff0000u);
    return r;
}
__device__ __forceinline__ float bf2f(unsigned short h) { return __builtin_bit_cast(float, (unsigned)h << 16); }

__global__ __launch_bounds__(512, 2)
void w8a8_kernel(const int* __restrict__ qx, const int* __restrict__ qw,
                 const unsigned short* __restrict__ sxp, const int* __restrict__ zxp,
                 const unsigned short* __restrict__ swp, const unsigned short* __restrict__ biasp,
                 unsigned short* __restrict__ out) {
    __shared__ __attribute__((aligned(16))) unsigned lds[(2 * 8192 + 8 * 2048 + 256) / 4];
    const unsigned long long t0_ = __builtin_amdgcn_s_memrealtime();
    const int tid = threadIdx.x;
    const int lane = tid & 63;
    const int wv = __builtin_amdgcn_readfirstlane(tid >> 6);
    const int bid = blockIdx.x;
    const int xcd = bid & 7, idx = bid >> 3;
    const int strip = xcd * 7 + (idx >> 2), mt = idx & 3;
    const int m0 = mt * BM, n0 = strip * BN;

    auto rx = __builtin_amdgcn_make_buffer_rsrc((void*)qx, 0, M_ * K_ * 4, 0x00020000);
    auto rw = __builtin_amdgcn_make_buffer_rsrc((void*)qw, 0, K_ * N_ * 4, 0x00020000);

    const int r8 = lane >> 3, q8 = lane & 7;
    const int g = lane >> 4, c = lane & 15;
    const unsigned xoff = ((m0 + 16 * wv + r8) * K_ + 4 * q8) * 4;
    const unsigned woff = ((32 * g + 4 * wv) * N_ + n0 + 4 * c) * 4;

    v4u xr[NBLK][8], wr[NBLK][4];
    const int rot = 2 * mt + (idx >> 2);
    auto issue = [&](int b) {
        const int bb = (b + rot) & 7;
#pragma unroll
        for (int j = 0; j < 4; ++j) {
            if (b < 2) wr[b][j] = __builtin_amdgcn_raw_buffer_load_b128(rw, woff, (128 * bb + j) * N_ * 4, NTAUX);
            else wr[b][j] = __builtin_amdgcn_raw_buffer_load_b128(rw, woff, (128 * bb + j) * N_ * 4, 0);
        }
#pragma unroll
        for (int hh = 0; hh < 2; ++hh)
#pragma unroll
            for (int i = 0; i < 4; ++i)
                xr[b][hh * 4 + i] = __builtin_amdgcn_raw_buffer_load_b128(rx, xoff + i * 128, hh * 8 * 4096 + bb * 512, 0);
    };
#pragma unroll
    for (int b = 0; b < PF; ++b) issue(b);

    const v2u swv = *(const v2u*)(swp + n0 + 4 * c);
    const v2u biv = *(const v2u*)(biasp + n0 + 4 * c);
    const float sx = bf2f(sxp[0]);
    const int zx = zxp[0];

    unsigned* csum = lds + (2 * 8192 + 8 * 2048) / 4;
    if (tid < 64) csum[tid] = 0;

    char* ldsb = (char*)lds;
    char* xa = ldsb + 2 * 8192 + wv * 2048;
    v4i acc[4];
#pragma unroll
    for (int t = 0; t < 4; ++t) acc[t] = (v4i){0, 0, 0, 0};
    int cs[4] = {0, 0, 0, 0};

#pragma unroll
    for (int b = 0; b < NBLK; ++b) {
        char* wb = ldsb + (b & 1) * 8192;
#pragma unroll
        for (int t = 0; t < 4; ++t) {
            unsigned p = pack4(wr[b][0][t], wr[b][1][t], wr[b][2][t], wr[b][3][t]);
            cs[t] = __builtin_amdgcn_sdot4(p, 0x01010101, cs[t], false);
            *(unsigned*)(wb + (wv * 4 + t) * 256 + c * 16 + g * 4) = p;
        }
#pragma unroll
        for (int hh = 0; hh < 2; ++hh) {
            v4u p;
#pragma unroll
            for (int i = 0; i < 4; ++i) {
                v4u v = xr[b][hh * 4 + i];
                p[i] = pack4(v[0], v[1], v[2], v[3]);
            }
            *(v4u*)(xa + q8 * 256 + (((8 * hh + r8) ^ q8) * 16)) = p;
        }
        if (b + PF < NBLK) issue(b + PF);
        __syncthreads();
#pragma unroll
        for (int s = 0; s < 2; ++s) {
            const int q = 4 * s + g;
            v4i a = *(const v4i*)(xa + q * 256 + ((c ^ q) * 16));
#pragma unroll
            for (int t = 0; t < 4; ++t) {
                v4i bb = *(const v4i*)(wb + q * 1024 + t * 256 + c * 16);
                acc[t] = __builtin_amdgcn_mfma_i32_16x16x64_i8(a, bb, acc[t], 0, 0, 0);
            }
        }
    }
#pragma unroll
    for (int t = 0; t < 4; ++t) atomicAdd(&csum[4 * c + t], (unsigned)cs[t]);
    __syncthreads();
    v4i cst = *(const v4i*)(csum + 4 * c);

    const float zf = rnd2((v2f){(float)zx, 0.f}).x;
    v2f zc[2], sw[2], bi[2];
#pragma unroll
    for (int p = 0; p < 2; ++p) {
        v2f cf = rnd2((v2f){(float)cst[2 * p], (float)cst[2 * p + 1]});
        zc[p] = rnd2(cf * zf);
        sw[p].x = __builtin_bit_cast(float, swv[p] << 16); sw[p].y = __builtin_bit_cast(float, swv[p] & 0xffff0000u);
        bi[p].x = __builtin_bit_cast(float, biv[p] << 16); bi[p].y = __builtin_bit_cast(float, biv[p] & 0xffff0000u);
    }
#pragma unroll
    for (int r = 0; r < 4; ++r) {
        v2u o;
#pragma unroll
        for (int p = 0; p < 2; ++p) {
            v2f v = rnd2((v2f){(float)acc[2 * p][r], (float)acc[2 * p + 1][r]});
            v = rnd2(v - zc[p]);
            v = rnd2(v * sx);
            v = rnd2(v * sw[p]);
            v = v + bi[p];
            o[p] = __builtin_bit_cast(unsigned, __builtin_convertvector(v, v2bf));
        }
        const int row = m0 + 16 * wv + 4 * g + r;
        __builtin_amdgcn_raw_buffer_store_b64(o, __builtin_amdgcn_make_buffer_rsrc((void*)out, 0, M_ * N_ * 2, 0x00020000), (row * N_ + n0 + 4 * c) * 2, 0, 16);
    }
    asm volatile("s_waitcnt vmcnt(0)" ::: "memory");
    __syncthreads();

    const unsigned long long tend = t0_ + 4000;
    while (__builtin_amdgcn_s_memrealtime() < tend) __builtin_amdgcn_s_sleep(1);
}

extern "C" void kernel_launch(void* const* d_in, const int* in_sizes, int n_in,
                              void* d_out, int out_size, void* d_ws, size_t ws_size,
                              hipStream_t stream) {
    w8a8_kernel<<<224, 512, 0, stream>>>((const int*)d_in[0], (const int*)d_in[1],
                                          (const unsigned short*)d_in[2], (const int*)d_in[3],
                                          (const unsigned short*)d_in[4], (const unsigned short*)d_in[5],
                                          (unsigned short*)d_out);
}
```

```cpp
#include <hip/hip_runtime.h>
#include <hip/hip_bf16.h>
#include <stdint.h>

typedef int v4i __attribute__((ext_vector_type(4)));
typedef unsigned v4u __attribute__((ext_vector_type(4)));
typedef unsigned v2u __attribute__((ext_vector_type(2)));
typedef float v2f __attribute__((ext_vector_type(2)));
typedef __bf16 v2bf __attribute__((ext_vector_type(2)));
typedef unsigned long long u64;
typedef __attribute__((address_space(1))) u64 gu64;

constexpr int M_ = 512, K_ = 1024, N_ = 3584;
constexpr int BM = 128, BN = 64;
constexpr unsigned FLAG_STRIDE = 128, WS_FLAG = 4096, WS_X8 = 65536;
constexpr u64 TAG_STEP = 0x9E3779B97F4A7C15ull;
constexpr int SC1 = 16;
constexpr unsigned SPIN_CAP = 1u << 15;
constexpr int LDS_CS = 65536, LDS_TAG = 65536 + 256, LDS_XT = 65536 + 512, LDS_BYTES = 65536 + 512 + 2048;

#define STAMP(i) do {} while (0)
#define FENCE() do { asm volatile("" ::: "memory"); __builtin_amdgcn_sched_barrier(0); } while (0)
#define VMWAIT(n) do { asm volatile("s_waitcnt vmcnt(" #n ")" ::: "memory"); __builtin_amdgcn_sched_barrier(0); } while (0)
#define BAR() do { asm volatile("s_waitcnt lgkmcnt(0)\n\ts_barrier" ::: "memory"); __builtin_amdgcn_sched_barrier(0); } while (0)

__device__ __forceinline__ unsigned pack4(unsigned a, unsigned b, unsigned c, unsigned d) {
    unsigned lo = __builtin_amdgcn_perm(b, a, 0x0c0c0400u);
    unsigned hi = __builtin_amdgcn_perm(d, c, 0x04000c0cu);
    return lo | hi;
}
__device__ __forceinline__ v2f rnd2(v2f v) {
    v2bf b = __builtin_convertvector(v, v2bf);
    unsigned u = __builtin_bit_cast(unsigned, b);
    v2f r; r.x = __builtin_bit_cast(float, u << 16); r.y = __builtin_bit_cast(float, u & 0xffff0000u);
    return r;
}
__device__ __forceinline__ float bf2f(unsigned short h) { return __builtin_bit_cast(float, (unsigned)h << 16); }
__device__ __forceinline__ u64 ld_agent(const void* p) { return __hip_atomic_load((gu64*)p, __ATOMIC_RELAXED, __HIP_MEMORY_SCOPE_AGENT); }
__device__ __forceinline__ void st_agent(void* p, u64 v) { __hip_atomic_store((gu64*)p, v, __ATOMIC_RELAXED, __HIP_MEMORY_SCOPE_AGENT); }

__global__ __launch_bounds__(576)
void w8a8_kernel(const int* __restrict__ qx, const int* __restrict__ qw,
                 const unsigned short* __restrict__ sxp, const int* __restrict__ zxp,
                 const unsigned short* __restrict__ swp, const unsigned short* __restrict__ biasp,
                 unsigned short* __restrict__ out, char* __restrict__ ws) {
    __shared__ __attribute__((aligned(16))) unsigned lds[LDS_BYTES / 4];
    asm volatile("" :: "s"(qx), "s"(qw), "s"(sxp), "s"(zxp), "s"(swp), "s"(biasp), "s"(out), "s"(ws));
    const int tid = threadIdx.x;
    const int lane = tid & 63;
    const int wv = __builtin_amdgcn_readfirstlane(tid >> 6) - 1;
    const int bid = blockIdx.x;
    const int xcd = bid & 7, idx = bid >> 3;
    const int g = lane >> 4, c = lane & 15;
    char* ldsb = (char*)lds;
    auto rx8 = __builtin_amdgcn_make_buffer_rsrc((void*)(ws + WS_X8), 0, M_ * K_, 0x00020000);

    if (wv < 0) {
        const int j = bid >> 3, kb = bid & 7;
        auto rx = __builtin_amdgcn_make_buffer_rsrc((void*)qx, 0, M_ * K_ * 4, 0x00020000);
        const int r8 = lane >> 3, q8 = lane & 7;
        const unsigned xoff = ((16 * j + r8) * K_ + 128 * kb + 4 * q8) * 4;
        v4u xr[8];
#pragma unroll
        for (int hh = 0; hh < 2; ++hh)
#pragma unroll
            for (int i = 0; i < 4; ++i)
                xr[hh * 4 + i] = __builtin_amdgcn_raw_buffer_load_b128(rx, xoff + i * 128, hh * 8 * 4096, 0);
        const u64 tag = ld_agent(ws) + TAG_STEP;
        lds[LDS_CS / 4 + lane] = 0;
        char* xa = ldsb + LDS_XT;
#pragma unroll
        for (int hh = 0; hh < 2; ++hh) {
            v4u p;
#pragma unroll
            for (int i = 0; i < 4; ++i) { v4u v = xr[hh * 4 + i]; p[i] = pack4(v[0], v[1], v[2], v[3]); }
            *(v4u*)(xa + q8 * 256 + (((8 * hh + r8) ^ q8) * 16)) = p;
        }
#pragma unroll
        for (int s = 0; s < 2; ++s) {
            const int q = 4 * s + g;
            v4u a = *(const v4u*)(xa + q * 256 + ((c ^ q) * 16));
            __builtin_amdgcn_raw_buffer_store_b128(a, rx8, ((j * 16 + 2 * kb + s) * 64 + lane) * 16, 0, SC1);
        }
        if (lane == 0) *(u64*)(ldsb + LDS_TAG) = tag;
        STAMP(2);
        VMWAIT(0);
        STAMP(3);
        if (lane == 0) st_agent(ws + WS_FLAG + bid * FLAG_STRIDE, tag);
        BAR();
        if (bid == 0) {
            for (unsigned spin = 0; spin < SPIN_CAP; ++spin) {
                bool ok = true;
#pragma unroll
                for (int k = 0; k < 4; ++k) ok &= (ld_agent(ws + WS_FLAG + (k * 64 + lane) * FLAG_STRIDE) == tag);
                if (__all(ok)) break;
                __builtin_amdgcn_s_sleep(4);
            }
            if (lane == 0) st_agent(ws, tag);
        }
        return;
    }
    if (idx < 4) {
        return;
    }

    const int gi = idx - 4;
    const int strip = xcd * 7 + (gi >> 2), mt = gi & 3;
    const int m0 = mt * BM, n0 = strip * BN;
    auto rw = __builtin_amdgcn_make_buffer_rsrc((void*)qw, 0, K_ * N_ * 4, 0x00020000);
    const unsigned woff = ((32 * g + 4 * wv) * N_ + n0 + 4 * c) * 4;
    constexpr int DORD[8] = {0, 1, 2, 4, 6, 3, 5, 7};
    auto blk = [&](int d) { return (2 * mt + DORD[d]) & 7; };

    v4u wr[8][4];
    auto ldw = [&](int d, int j) { wr[d][j] = __builtin_amdgcn_raw_buffer_load_b128(rw, woff, (128 * blk(d) + j) * N_ * 4, 0); };
    auto ldw4 = [&](int d) { ldw(d, 0); ldw(d, 1); ldw(d, 2); ldw(d, 3); };
    int cs[4] = {0, 0, 0, 0};
    auto pack_w = [&](int d) {
        char* wb = ldsb + d * 8192;
#pragma unroll
        for (int t = 0; t < 4; ++t) {
            unsigned p = pack4(wr[d][0][t], wr[d][1][t], wr[d][2][t], wr[d][3][t]);
            cs[t] = __builtin_amdgcn_sdot4(p, 0x01010101, cs[t], false);
            *(unsigned*)(wb + (wv * 4 + t) * 256 + c * 16 + g * 4) = p;
        }
    };
    v4i xa[8][2];
    const unsigned x8off = ((mt * 8 + wv) * 16 * 64 + lane) * 16;
    auto ldx = [&](int d) {
#pragma unroll
        for (int s = 0; s < 2; ++s)
            xa[d][s] = __builtin_bit_cast(v4i, __builtin_amdgcn_raw_buffer_load_b128(rx8, x8off + s * 1024, blk(d) * 2048, SC1));
    };
    v4i acc[4];
#pragma unroll
    for (int t = 0; t < 4; ++t) acc[t] = (v4i){0, 0, 0, 0};
    v4i bf[16][4];
    auto ldb = [&](int u) {
#pragma unroll
        for (int t = 0; t < 4; ++t)
            bf[u][t] = *(const v4i*)(ldsb + u * 4096 + g * 1024 + t * 256 + c * 16);
    };
    auto mma = [&](int u) {
#pragma unroll
        for (int t = 0; t < 4; ++t)
            acc[t] = __builtin_amdgcn_mfma_i32_16x16x64_i8(xa[u >> 1][u & 1], bf[u][t], acc[t], 0, 0, 0);
    };
    auto step = [&](int u, int pf) { if (pf >= 0) ldb(pf); FENCE(); mma(u); FENCE(); };

    ldw4(0);
    FENCE();
    auto rsw = __builtin_amdgcn_make_buffer_rsrc((void*)swp, 0, N_ * 2, 0x00020000);
    auto rbi = __builtin_amdgcn_make_buffer_rsrc((void*)biasp, 0, N_ * 2, 0x00020000);
    auto rsx = __builtin_amdgcn_make_buffer_rsrc((void*)sxp, 0, 2, 0x00020000);
    auto rzx = __builtin_amdgcn_make_buffer_rsrc((void*)zxp, 0, 4, 0x00020000);
    const v2u swv = __builtin_amdgcn_raw_buffer_load_b64(rsw, (n0 + 4 * c) * 2, 0, 0);
    const v2u biv = __builtin_amdgcn_raw_buffer_load_b64(rbi, (n0 + 4 * c) * 2, 0, 0);
    const unsigned short sxh = __builtin_amdgcn_raw_buffer_load_b16(rsx, 0, 0, 0);
    const int zx = __builtin_amdgcn_raw_buffer_load_b32(rzx, 0, 0, 0);
    FENCE();
    VMWAIT(7); ldw(1, 0); FENCE();
    VMWAIT(7); ldw(1, 1); FENCE();
    VMWAIT(7); ldw(1, 2); FENCE();
    VMWAIT(7); ldw(1, 3); FENCE();
    pack_w(0); FENCE();
    STAMP(2);
    VMWAIT(3); ldw(2, 0); ldw(2, 1); FENCE();
    VMWAIT(4); ldw(2, 2); ldw(2, 3); FENCE();
    VMWAIT(5); ldw(3, 0); ldw(3, 1); FENCE();
    VMWAIT(6); ldw(3, 2); ldw(3, 3); FENCE();
    pack_w(1); FENCE();
    STAMP(3);
    const char* flagp = ws + WS_FLAG + ((mt * 8 + wv) * 8 + (lane & 7)) * FLAG_STRIDE;
    VMWAIT(4);
    u64 fl = ld_agent(flagp);
    FENCE();
    ldw4(4); FENCE();
    pack_w(2); FENCE();
    VMWAIT(5); ldw4(5); FENCE();
    pack_w(3); FENCE();
    BAR();
    const u64 tag = *(const u64*)(ldsb + LDS_TAG);
    if (!__all(fl == tag)) {
        for (unsigned spin = 0; spin < SPIN_CAP; ++spin) {
            __builtin_amdgcn_s_sleep(4);
            if (__all(ld_agent(flagp) == tag)) break;
        }
    }
    FENCE();
    STAMP(4);
    ldx(0); ldx(1); FENCE();
    pack_w(4); FENCE();
    ldw4(6); ldx(2); ldx(3); FENCE();
    pack_w(5); FENCE();
    ldw4(7); ldx(4); ldx(5); FENCE();
    BAR();
    STAMP(5);
    ldb(0); FENCE();
    step(0, 1); step(1, 2);
    ldx(6); FENCE();
    step(2, 3); step(3, 4);
    ldx(7); FENCE();
    pack_w(6); FENCE();
    step(4, 5); step(5, 6); step(6, 7); step(7, 8);
    pack_w(7);
#pragma unroll
    for (int t = 0; t < 4; ++t) atomicAdd(&lds[LDS_CS / 4 + 4 * c + t], (unsigned)cs[t]);
    FENCE();
    step(8, 9); step(9, 10); step(10, 11);
    BAR();
    step(11, 12); step(12, 13); step(13, 14);
    v4i cst = *(const v4i*)(ldsb + LDS_CS + 16 * c);
    step(14, 15); step(15, -1);
    STAMP(6);
    const float sx = bf2f(sxh);

    const float zf = rnd2((v2f){(float)zx, 0.f}).x;
    v2f zc[2], sw[2], bi[2];
#pragma unroll
    for (int p = 0; p < 2; ++p) {
        v2f cf = rnd2((v2f){(float)cst[2 * p], (float)cst[2 * p + 1]});
        zc[p] = rnd2(cf * zf);
        sw[p].x = __builtin_bit_cast(float, swv[p] << 16); sw[p].y = __builtin_bit_cast(float, swv[p] & 0xffff0000u);
        bi[p].x = __builtin_bit_cast(float, biv[p] << 16); bi[p].y = __builtin_bit_cast(float, biv[p] & 0xffff0000u);
    }
    auto ro = __builtin_amdgcn_make_buffer_rsrc((void*)out, 0, M_ * N_ * 2, 0x00020000);
    const unsigned ooff = ((m0 + 16 * wv + 4 * g) * N_ + n0 + 4 * c) * 2;
#pragma unroll
    for (int r = 0; r < 4; ++r) {
        v2u o;
#pragma unroll
        for (int p = 0; p < 2; ++p) {
            v2f v = rnd2((v2f){(float)acc[2 * p][r], (float)acc[2 * p + 1][r]});
            v = rnd2(v - zc[p]);
            v = rnd2(v * sx);
            v = rnd2(v * sw[p]);
            v = v + bi[p];
            o[p] = __builtin_bit_cast(unsigned, __builtin_convertvector(v, v2bf));
        }
        __builtin_amdgcn_raw_buffer_store_b64(o, ro, ooff, r * N_ * 2, SC1);
    }
    STAMP(7);
}

extern "C" void kernel_launch(void* const* d_in, const int* in_sizes, int n_in,
                              void* d_out, int out_size, void* d_ws, size_t ws_size,
                              hipStream_t stream) {
    w8a8_kernel<<<256, 576, 0, stream>>>((const int*)d_in[0], (const int*)d_in[1],
                                          (const unsigned short*)d_in[2], (const int*)d_in[3],
                                          (const unsigned short*)d_in[4], (const unsigned short*)d_in[5],
                                          (unsigned short*)d_out, (char*)d_ws);
}
```

```cpp
#include <hip/hip_runtime.h>
#include <hip/hip_bf16.h>
#include <stdint.h>

typedef int v4i __attribute__((ext_vector_type(4)));
typedef unsigned v4u __attribute__((ext_vector_type(4)));
typedef unsigned v2u __attribute__((ext_vector_type(2)));
typedef float v2f __attribute__((ext_vector_type(2)));
typedef __bf16 v2bf __attribute__((ext_vector_type(2)));
typedef unsigned long long u64;
typedef __attribute__((address_space(1))) u64 gu64;

constexpr int M_ = 512, K_ = 1024, N_ = 3584;
constexpr int BM = 128, BN = 64;
constexpr unsigned FLAG_STRIDE = 128, WS_FLAG = 4096, WS_X8 = 65536;
constexpr u64 TAG_STEP = 0x9E3779B97F4A7C15ull;
constexpr int SC1 = 16;
constexpr unsigned SPIN_CAP = 1u << 15;
constexpr int LDS_CS = 65536, LDS_TAG = 65536 + 256, LDS_XT = 65536 + 512, LDS_BYTES = 65536 + 512 + 2048;

#define STAMP(i) do {} while (0)
#define FENCE() do { asm volatile("" ::: "memory"); __builtin_amdgcn_sched_barrier(0); } while (0)
#define VMWAIT(n) do { asm volatile("s_waitcnt vmcnt(" #n ")" ::: "memory"); __builtin_amdgcn_sched_barrier(0); } while (0)
#define BAR() do { asm volatile("s_waitcnt lgkmcnt(0)\n\ts_barrier" ::: "memory"); __builtin_amdgcn_sched_barrier(0); } while (0)

__device__ __forceinline__ unsigned pack4(unsigned a, unsigned b, unsigned c, unsigned d) {
    unsigned lo = __builtin_amdgcn_perm(b, a, 0x0c0c0400u);
    unsigned hi = __builtin_amdgcn_perm(d, c, 0x04000c0cu);
    return lo | hi;
}
__device__ __forceinline__ v2f rnd2(v2f v) {
    v2bf b = __builtin_convertvector(v, v2bf);
    unsigned u = __builtin_bit_cast(unsigned, b);
    v2f r; r.x = __builtin_bit_cast(float, u << 16); r.y = __builtin_bit_cast(float, u & 0xffff0000u);
    return r;
}
__device__ __forceinline__ float bf2f(unsigned short h) { return __builtin_bit_cast(float, (unsigned)h << 16); }
__device__ __forceinline__ u64 ld_agent(const void* p) { return __hip_atomic_load((gu64*)p, __ATOMIC_RELAXED, __HIP_MEMORY_SCOPE_AGENT); }
__device__ __forceinline__ void st_agent(void* p, u64 v) { __hip_atomic_store((gu64*)p, v, __ATOMIC_RELAXED, __HIP_MEMORY_SCOPE_AGENT); }

__global__ __launch_bounds__(576)
void w8a8_kernel(const int* __restrict__ qx, const int* __restrict__ qw,
                 const unsigned short* __restrict__ sxp, const int* __restrict__ zxp,
                 const unsigned short* __restrict__ swp, const unsigned short* __restrict__ biasp,
                 unsigned short* __restrict__ out, char* __restrict__ ws) {
    __shared__ __attribute__((aligned(16))) unsigned lds[LDS_BYTES / 4];
    asm volatile("" :: "s"(qx), "s"(qw), "s"(sxp), "s"(zxp), "s"(swp), "s"(biasp), "s"(out), "s"(ws));
    const int tid = threadIdx.x;
    const int lane = tid & 63;
    const int wv = __builtin_amdgcn_readfirstlane(tid >> 6) - 1;
    const int bid = blockIdx.x;
    const int xcd = bid & 7, idx = bid >> 3;
    const int g = lane >> 4, c = lane & 15;
    char* ldsb = (char*)lds;
    auto rx8 = __builtin_amdgcn_make_buffer_rsrc((void*)(ws + WS_X8), 0, M_ * K_, 0x00020000);

    if (wv < 0) {
        const int j = bid >> 3, kb = bid & 7;
        auto rx = __builtin_amdgcn_make_buffer_rsrc((void*)qx, 0, M_ * K_ * 4, 0x00020000);
        const int r8 = lane >> 3, q8 = lane & 7;
        const unsigned xoff = ((16 * j + r8) * K_ + 128 * kb + 4 * q8) * 4;
        v4u xr[8];
#pragma unroll
        for (int hh = 0; hh < 2; ++hh)
#pragma unroll
            for (int i = 0; i < 4; ++i)
                xr[hh * 4 + i] = __builtin_amdgcn_raw_buffer_load_b128(rx, xoff + i * 128, hh * 8 * 4096, 0);
        const u64 tag = ld_agent(ws) + TAG_STEP;
        if (idx >= 4) {
            const int gi = idx - 4, strip = xcd * 7 + (gi >> 2), mt = gi & 3;
            auto ro = __builtin_amdgcn_make_buffer_rsrc((void*)out, 0, M_ * N_ * 2, 0x00020000);
            unsigned tv = __builtin_amdgcn_raw_buffer_load_b32(ro, ((mt * BM + (lane & 1) * 127) * N_ + strip * BN) * 2, 0, 0);
            asm volatile("" :: "v"(tv));
        }
        lds[LDS_CS / 4 + lane] = 0;
        char* xa = ldsb + LDS_XT;
#pragma unroll
        for (int hh = 0; hh < 2; ++hh) {
            v4u p;
#pragma unroll
            for (int i = 0; i < 4; ++i) { v4u v = xr[hh * 4 + i]; p[i] = pack4(v[0], v[1], v[2], v[3]); }
            *(v4u*)(xa + q8 * 256 + (((8 * hh + r8) ^ q8) * 16)) = p;
        }
#pragma unroll
        for (int s = 0; s < 2; ++s) {
            const int q = 4 * s + g;
            v4u a = *(const v4u*)(xa + q * 256 + ((c ^ q) * 16));
            __builtin_amdgcn_raw_buffer_store_b128(a, rx8, ((j * 16 + 2 * kb + s) * 64 + lane) * 16, 0, SC1);
        }
        if (lane == 0) *(u64*)(ldsb + LDS_TAG) = tag;
        STAMP(2);
        VMWAIT(0);
        STAMP(3);
        if (lane == 0) st_agent(ws + WS_FLAG + bid * FLAG_STRIDE, tag);
                {
            const char* fp = ws + WS_FLAG + (idx * 8 + (lane & 7)) * FLAG_STRIDE;
            for (unsigned spin = 0; spin < SPIN_CAP; ++spin) {
                if (__all(ld_agent(fp) == tag)) break;
                __builtin_amdgcn_s_sleep(2);
            }
            FENCE();
            unsigned t0v = __builtin_amdgcn_raw_buffer_load_b32(rx8, idx * 16384 + lane * 128, 0, SC1);
            unsigned t1v = __builtin_amdgcn_raw_buffer_load_b32(rx8, idx * 16384 + lane * 128, 8192, SC1);
            asm volatile("" :: "v"(t0v), "v"(t1v));
        }
        BAR();
        if (bid == 0) {
            for (unsigned spin = 0; spin < SPIN_CAP; ++spin) {
                bool ok = true;
#pragma unroll
                for (int k = 0; k < 4; ++k) ok &= (ld_agent(ws + WS_FLAG + (k * 64 + lane) * FLAG_STRIDE) == tag);
                if (__all(ok)) break;
                __builtin_amdgcn_s_sleep(4);
            }
            if (lane == 0) st_agent(ws, tag);
        }
        return;
    }
    if (idx < 4) {
        return;
    }

    const int gi = idx - 4;
    const int strip = xcd * 7 + (gi >> 2), mt = gi & 3;
    const int m0 = mt * BM, n0 = strip * BN;
    auto rw = __builtin_amdgcn_make_buffer_rsrc((void*)qw, 0, K_ * N_ * 4, 0x00020000);
    const unsigned woff = ((32 * g + 4 * wv) * N_ + n0 + 4 * c) * 4;
    constexpr int DORD[8] = {0, 1, 2, 4, 6, 3, 5, 7};
    auto blk = [&](int d) { return (2 * mt + DORD[d]) & 7; };

    v4u wr[8][4];
    auto ldw = [&](int d, int j) { wr[d][j] = __builtin_amdgcn_raw_buffer_load_b128(rw, woff, (128 * blk(d) + j) * N_ * 4, 0); };
    auto ldw4 = [&](int d) { ldw(d, 0); ldw(d, 1); ldw(d, 2); ldw(d, 3); };
    int cs[4] = {0, 0, 0, 0};
    auto pack_w = [&](int d) {
        char* wb = ldsb + d * 8192;
#pragma unroll
        for (int t = 0; t < 4; ++t) {
            unsigned p = pack4(wr[d][0][t], wr[d][1][t], wr[d][2][t], wr[d][3][t]);
            cs[t] = __builtin_amdgcn_sdot4(p, 0x01010101, cs[t], false);
            *(unsigned*)(wb + (wv * 4 + t) * 256 + c * 16 + g * 4) = p;
        }
    };
    v4i xa[8][2];
    const unsigned x8off = ((mt * 8 + wv) * 16 * 64 + lane) * 16;
    auto ldx = [&](int d) {
#pragma unroll
        for (int s = 0; s < 2; ++s)
            xa[d][s] = __builtin_bit_cast(v4i, __builtin_amdgcn_raw_buffer_load_b128(rx8, x8off + s * 1024, blk(d) * 2048, SC1));
    };
    v4i acc[4];
#pragma unroll
    for (int t = 0; t < 4; ++t) acc[t] = (v4i){0, 0, 0, 0};
    v4i bf[16][4];
    auto ldb = [&](int u) {
#pragma unroll
        for (int t = 0; t < 4; ++t)
            bf[u][t] = *(const v4i*)(ldsb + u * 4096 + g * 1024 + t * 256 + c * 16);
    };
    auto mma = [&](int u) {
#pragma unroll
        for (int t = 0; t < 4; ++t)
            acc[t] = __builtin_amdgcn_mfma_i32_16x16x64_i8(xa[u >> 1][u & 1], bf[u][t], acc[t], 0, 0, 0);
    };
    auto step = [&](int u, int pf) { if (pf >= 0) ldb(pf); FENCE(); mma(u); FENCE(); };

    ldw4(0);
    FENCE();
    auto rsw = __builtin_amdgcn_make_buffer_rsrc((void*)swp, 0, N_ * 2, 0x00020000);
    auto rbi = __builtin_amdgcn_make_buffer_rsrc((void*)biasp, 0, N_ * 2, 0x00020000);
    auto rsx = __builtin_amdgcn_make_buffer_rsrc((void*)sxp, 0, 2, 0x00020000);
    auto rzx = __builtin_amdgcn_make_buffer_rsrc((void*)zxp, 0, 4, 0x00020000);
    const v2u swv = __builtin_amdgcn_raw_buffer_load_b64(rsw, (n0 + 4 * c) * 2, 0, 0);
    const v2u biv = __builtin_amdgcn_raw_buffer_load_b64(rbi, (n0 + 4 * c) * 2, 0, 0);
    const unsigned short sxh = __builtin_amdgcn_raw_buffer_load_b16(rsx, 0, 0, 0);
    const int zx = __builtin_amdgcn_raw_buffer_load_b32(rzx, 0, 0, 0);
    FENCE();
    VMWAIT(7); ldw(1, 0); FENCE();
    VMWAIT(7); ldw(1, 1); FENCE();
    VMWAIT(7); ldw(1, 2); FENCE();
    VMWAIT(7); ldw(1, 3); FENCE();
    pack_w(0); FENCE();
    STAMP(2);
    VMWAIT(3); ldw(2, 0); ldw(2, 1); FENCE();
    VMWAIT(4); ldw(2, 2); ldw(2, 3); FENCE();
    VMWAIT(5); ldw(3, 0); ldw(3, 1); FENCE();
    VMWAIT(6); ldw(3, 2); ldw(3, 3); FENCE();
    pack_w(1); FENCE();
    STAMP(3);
    const char* flagp = ws + WS_FLAG + ((mt * 8 + wv) * 8 + (lane & 7)) * FLAG_STRIDE;
    VMWAIT(4);
    u64 fl = ld_agent(flagp);
    FENCE();
    ldw4(4); FENCE();
    pack_w(2); FENCE();
    VMWAIT(5); ldw4(5); FENCE();
    pack_w(3); FENCE();
    BAR();
    const u64 tag = *(const u64*)(ldsb + LDS_TAG);
    if (!__all(fl == tag)) {
        for (unsigned spin = 0; spin < SPIN_CAP; ++spin) {
            __builtin_amdgcn_s_sleep(4);
            if (__all(ld_agent(flagp) == tag)) break;
        }
    }
    FENCE();
    STAMP(4);
    ldx(0); ldx(1); FENCE();
    pack_w(4); FENCE();
    ldw4(6); ldx(2); ldx(3); FENCE();
    pack_w(5); FENCE();
    ldw4(7); ldx(4); ldx(5); FENCE();
    BAR();
    STAMP(5);
    ldb(0); FENCE();
    step(0, 1); step(1, 2);
    ldx(6); FENCE();
    step(2, 3); step(3, 4);
    ldx(7); FENCE();
    pack_w(6); FENCE();
    step(4, 5); step(5, 6); step(6, 7); step(7, 8);
    pack_w(7);
#pragma unroll
    for (int t = 0; t < 4; ++t) atomicAdd(&lds[LDS_CS / 4 + 4 * c + t], (unsigned)cs[t]);
    FENCE();
    step(8, 9); step(9, 10); step(10, 11);
    BAR();
    step(11, 12); step(12, 13); step(13, 14);
    v4i cst = *(const v4i*)(ldsb + LDS_CS + 16 * c);
    step(14, 15); step(15, -1);
    STAMP(6);
    const float sx = bf2f(sxh);

    const float zf = rnd2((v2f){(float)zx, 0.f}).x;
    v2f sc[2], of[2];
#pragma unroll
    for (int p = 0; p < 2; ++p) {
        v2f cf = rnd2((v2f){(float)cst[2 * p], (float)cst[2 * p + 1]});
        v2f zc = rnd2(cf * zf), sw, bi;
        sw.x = __builtin_bit_cast(float, swv[p] << 16); sw.y = __builtin_bit_cast(float, swv[p] & 0xffff0000u);
        bi.x = __builtin_bit_cast(float, biv[p] << 16); bi.y = __builtin_bit_cast(float, biv[p] & 0xffff0000u);
        sc[p] = sw * sx;
        of[p] = bi - zc * sc[p];
    }
    auto ro = __builtin_amdgcn_make_buffer_rsrc((void*)out, 0, M_ * N_ * 2, 0x00020000);
    const unsigned ooff = ((m0 + 16 * wv + 4 * g) * N_ + n0 + 4 * c) * 2;
#pragma unroll
    for (int r = 0; r < 4; ++r) {
        v2u o;
#pragma unroll
        for (int p = 0; p < 2; ++p) {
            v2f v = (v2f){(float)acc[2 * p][r], (float)acc[2 * p + 1][r]} * sc[p] + of[p];
            o[p] = __builtin_bit_cast(unsigned, __builtin_convertvector(v, v2bf));
        }
        __builtin_amdgcn_raw_buffer_store_b64(o, ro, ooff, r * N_ * 2, SC1);
    }
    STAMP(7);
}

extern "C" void kernel_launch(void* const* d_in, const int* in_sizes, int n_in,
                              void* d_out, int out_size, void* d_ws, size_t ws_size,
                              hipStream_t stream) {
    w8a8_kernel<<<256, 576, 0, stream>>>((const int*)d_in[0], (const int*)d_in[1],
                                          (const unsigned short*)d_in[2], (const int*)d_in[3],
                                          (const unsigned short*)d_in[4], (const unsigned short*)d_in[5],
                                          (unsigned short*)d_out, (char*)d_ws);
}
```

```cpp
#include <hip/hip_runtime.h>
#include <hip/hip_bf16.h>
#include <stdint.h>

typedef int v4i __attribute__((ext_vector_type(4)));
typedef unsigned v4u __attribute__((ext_vector_type(4)));
typedef unsigned v2u __attribute__((ext_vector_type(2)));
typedef float v2f __attribute__((ext_vector_type(2)));
typedef __bf16 v2bf __attribute__((ext_vector_type(2)));
typedef unsigned long long u64;
typedef __attribute__((address_space(1))) u64 gu64;
typedef __attribute__((address_space(3))) volatile unsigned lds_vu32;

constexpr int M_ = 512, K_ = 1024, N_ = 3584;
constexpr int BM = 128, BN = 64;
constexpr unsigned FS = 128;
constexpr unsigned WS_XFLAG = 4096;
constexpr unsigned WS_WFLAG = WS_XFLAG + 256 * FS;
constexpr unsigned WS_X8 = 262144;
constexpr unsigned WS_W8 = WS_X8 + M_ * K_;
constexpr u64 TAG_STEP = 0x9E3779B97F4A7C15ull;
constexpr int SC1 = 16;
constexpr unsigned SPIN_CAP = 1u << 15;
constexpr int LDS_CS = 65536, LDS_CTL = LDS_CS + 256, LDS_XT = LDS_CTL + 256, LDS_BYTES = LDS_XT + 2048;
constexpr int NT = 9 * 64;
#define POLL_SLEEP 2

#define STAMP(i) do {} while (0)
#define STAMP_OUT() do {} while (0)
#define FENCE() do { asm volatile("" ::: "memory"); __builtin_amdgcn_sched_barrier(0); } while (0)
#define VMWAIT(n) do { asm volatile("s_waitcnt vmcnt(" #n ")" ::: "memory"); __builtin_amdgcn_sched_barrier(0); } while (0)
#define LGKM0() do { asm volatile("s_waitcnt lgkmcnt(0)" ::: "memory"); __builtin_amdgcn_sched_barrier(0); } while (0)
#define BAR() do { asm volatile("s_waitcnt lgkmcnt(0)\n\ts_barrier" ::: "memory"); __builtin_amdgcn_sched_barrier(0); } while (0)

__device__ __forceinline__ unsigned pack4(unsigned a, unsigned b, unsigned c, unsigned d) {
    unsigned lo = __builtin_amdgcn_perm(b, a, 0x0c0c0400u);
    unsigned hi = __builtin_amdgcn_perm(d, c, 0x04000c0cu);
    return lo | hi;
}
__device__ __forceinline__ v2f rnd2(v2f v) {
    v2bf b = __builtin_convertvector(v, v2bf);
    unsigned u = __builtin_bit_cast(unsigned, b);
    v2f r; r.x = __builtin_bit_cast(float, u << 16); r.y = __builtin_bit_cast(float, u & 0xffff0000u);
    return r;
}
__device__ __forceinline__ float bf2f(unsigned short h) { return __builtin_bit_cast(float, (unsigned)h << 16); }
__device__ __forceinline__ u64 ld_agent(const void* p) { return __hip_atomic_load((gu64*)p, __ATOMIC_RELAXED, __HIP_MEMORY_SCOPE_AGENT); }
__device__ __forceinline__ void st_agent(void* p, u64 v) { __hip_atomic_store((gu64*)p, v, __ATOMIC_RELAXED, __HIP_MEMORY_SCOPE_AGENT); }
__device__ __forceinline__ void lds_wait_ge(lds_vu32* w, unsigned v) {
#pragma nounroll
    for (unsigned spin = 0; spin < (SPIN_CAP << 4); ++spin) {
        if ((unsigned)__builtin_amdgcn_readfirstlane(*w) >= v) break;
        __builtin_amdgcn_s_sleep(1);
    }
    FENCE();
}
__device__ __forceinline__ void poll_flags(const char* p, u64 tag) {
#pragma nounroll
    for (unsigned spin = 0; spin < SPIN_CAP; ++spin) {
        if (__all(ld_agent(p) == tag)) break;
        __builtin_amdgcn_s_sleep(POLL_SLEEP);
    }
    FENCE();
}

#define GATE 6
__global__ __launch_bounds__(NT)
void w8a8_kernel(const int* __restrict__ qx, const int* __restrict__ qw,
                 const unsigned short* __restrict__ sxp, const int* __restrict__ zxp,
                 const unsigned short* __restrict__ swp, const unsigned short* __restrict__ biasp,
                 unsigned short* __restrict__ out, char* __restrict__ ws) {
    __shared__ __attribute__((aligned(16))) unsigned lds[LDS_BYTES / 4];
    asm volatile("" :: "s"(qx), "s"(qw), "s"(sxp), "s"(zxp), "s"(swp), "s"(biasp), "s"(out), "s"(ws));
    const int tid = threadIdx.x;
    const int lane = tid & 63;
    const int wv = __builtin_amdgcn_readfirstlane(tid >> 6) - 1;
    const int bid = blockIdx.x;
    const int xcd = bid & 7, idx = bid >> 3;
    const int g = lane >> 4, c = lane & 15;
    char* ldsb = (char*)lds;
    lds_vu32* ctl = (lds_vu32*)(lds + LDS_CTL / 4);
    auto rx8 = __builtin_amdgcn_make_buffer_rsrc((void*)(ws + WS_X8), 0, M_ * K_, 0x00020000);
    const bool spare = idx < 4;
    const int sl = spare ? 7 : ((idx - 4) >> 2), mt = idx & 3;
    const int strip = xcd * 7 + sl;

    if (wv < 0) {
        const int j = bid >> 3, kb = bid & 7;
        auto rx = __builtin_amdgcn_make_buffer_rsrc((void*)qx, 0, M_ * K_ * 4, 0x00020000);
        const int r8 = lane >> 3, q8 = lane & 7;
        const unsigned xoff = ((16 * j + r8) * K_ + 128 * kb + 4 * q8) * 4;
        v4u xr[8];
#pragma unroll
        for (int hh = 0; hh < 2; ++hh)
#pragma unroll
            for (int i = 0; i < 4; ++i)
                xr[hh * 4 + i] = __builtin_amdgcn_raw_buffer_load_b128(rx, xoff + i * 128, hh * 8 * 4096, 0);
        const u64 tag = ld_agent(ws) + TAG_STEP;
        FENCE();
        lds[LDS_CS / 4 + lane] = 0;
        lds[LDS_CTL / 4 + lane] = 0;
        BAR();
        char* xa = ldsb + LDS_XT;
#pragma unroll
        for (int hh = 0; hh < 2; ++hh) {
            v4u p;
#pragma unroll
            for (int i = 0; i < 4; ++i) { v4u v = xr[hh * 4 + i]; p[i] = pack4(v[0], v[1], v[2], v[3]); }
            *(v4u*)(xa + q8 * 256 + (((8 * hh + r8) ^ q8) * 16)) = p;
        }
#pragma unroll
        for (int s = 0; s < 2; ++s) {
            const int q = 4 * s + g;
            v4u a = *(const v4u*)(xa + q * 256 + ((c ^ q) * 16));
            __builtin_amdgcn_raw_buffer_store_b128(a, rx8, ((j * 16 + 2 * kb + s) * 64 + lane) * 16, 0, SC1);
        }
        STAMP(2);
        VMWAIT(0);
        STAMP(3);
        if (lane == 0) st_agent(ws + WS_XFLAG + bid * FS, tag);
        VMWAIT(0);
        STAMP(4);
        poll_flags(ws + WS_XFLAG + ((mt * 8 + sl) * 8 + (lane & 7)) * FS, tag);
        const unsigned t0v = __builtin_amdgcn_raw_buffer_load_b32(rx8, (mt * 8 + sl) * 16384 + lane * 128, 0, SC1);
        const unsigned t1v = __builtin_amdgcn_raw_buffer_load_b32(rx8, (mt * 8 + sl) * 16384 + lane * 128, 8192, SC1);
        FENCE();
        STAMP(5);
        if (!spare) {
            poll_flags(ws + WS_XFLAG + (mt * 64 + lane) * FS, tag);
            ctl[3] = 1;
            STAMP(6);
        } else if (bid == 0) {
#pragma nounroll
            for (unsigned spin = 0; spin < SPIN_CAP; ++spin) {
                bool ok = true;
#pragma unroll
                for (int k = 0; k < 4; ++k) ok &= (ld_agent(ws + WS_XFLAG + (k * 64 + lane) * FS) == tag);
                if (__all(ok)) break;
                __builtin_amdgcn_s_sleep(8);
            }
            if (lane == 0) st_agent(ws, tag);
        }
        asm volatile("" :: "v"(t0v), "v"(t1v));
        STAMP_OUT();
        return;
    }
    if (spare) return;

    const int m0 = mt * BM, n0 = strip * BN;
    auto rsw = __builtin_amdgcn_make_buffer_rsrc((void*)swp, 0, N_ * 2, 0x00020000);
    auto rbi = __builtin_amdgcn_make_buffer_rsrc((void*)biasp, 0, N_ * 2, 0x00020000);
    auto rsx = __builtin_amdgcn_make_buffer_rsrc((void*)sxp, 0, 2, 0x00020000);
    auto rzx = __builtin_amdgcn_make_buffer_rsrc((void*)zxp, 0, 4, 0x00020000);
    auto rw = __builtin_amdgcn_make_buffer_rsrc((void*)qw, 0, K_ * N_ * 4, 0x00020000);
    const unsigned woff = (4 * g * N_ + n0 + 4 * c) * 4;
    const int sgA = (4 * mt + wv) & 15, sgB = (4 * mt + 8 + wv) & 15;
    const unsigned wbA = (128 * (sgA >> 1) + 16 * (sgA & 1)) * N_ * 4, wbB = (128 * (sgB >> 1) + 16 * (sgB & 1)) * N_ * 4;
    v4u wr[8][4], pk[4];
    int cs[4] = {0, 0, 0, 0};
    auto ld1 = [&](int n) { wr[n >> 2][n & 3] = __builtin_amdgcn_raw_buffer_load_b128(rw, woff, (n < 16 ? wbA : wbB) + (32 * ((n >> 2) & 3) + (n & 3)) * N_ * 4, 0); FENCE(); };
    auto packq = [&](int i) {
#pragma unroll
        for (int t = 0; t < 4; ++t) {
            unsigned p = pack4(wr[i][0][t], wr[i][1][t], wr[i][2][t], wr[i][3][t]);
            cs[t] = __builtin_amdgcn_sdot4(p, 0x01010101, cs[t], false);
            asm volatile("" : "+v"(p), "+v"(cs[t]));
            pk[t][i & 3] = p;
        }
        FENCE();
    };
    auto stash = [&](int slot) {
#pragma unroll
        for (int t = 0; t < 4; ++t) *(v4u*)(ldsb + slot * 4096 + t * 1024 + lane * 16) = pk[t];
        FENCE();
    };
    v4i xa[16];
    const unsigned x8off = ((mt * 8 + wv) * 16 * 64 + lane) * 16;
    auto ldx = [&](int p) { xa[p] = __builtin_bit_cast(v4i, __builtin_amdgcn_raw_buffer_load_b128(rx8, x8off, ((4 * mt + p) & 15) * 1024, SC1)); FENCE(); };

    const v2u swv = __builtin_amdgcn_raw_buffer_load_b64(rsw, (n0 + 4 * c) * 2, 0, 0);
    const v2u biv = __builtin_amdgcn_raw_buffer_load_b64(rbi, (n0 + 4 * c) * 2, 0, 0);
    const unsigned short sxh = __builtin_amdgcn_raw_buffer_load_b16(rsx, 0, 0, 0);
    const int zx = __builtin_amdgcn_raw_buffer_load_b32(rzx, 0, 0, 0);
    FENCE();
    if (wv >= 4) { BAR(); lds_wait_ge(ctl + 4, GATE); }
#pragma unroll
    for (int n = 0; n < 8; ++n) ld1(n);
    if (wv < 4) BAR();
#define RF(n) do { VMWAIT(7); ld1(n); } while (0)
    RF(8); RF(9); RF(10); RF(11); packq(0);
    RF(12); RF(13); RF(14); RF(15); packq(1);
    if (wv < 4 && lane == 0) atomicAdd(&lds[LDS_CTL / 4 + 4], 1u);
    RF(16); RF(17); RF(18); RF(19); packq(2);
    RF(20); RF(21); RF(22); RF(23); packq(3);
    if (wv < 4 && lane == 0) atomicAdd(&lds[LDS_CTL / 4 + 4], 1u);
    stash(wv);
    STAMP(2);
    RF(24); RF(25); RF(26); RF(27); packq(4);
    RF(28); RF(29); RF(30); RF(31); packq(5);
    STAMP(3);
    lds_wait_ge(ctl + 3, 1);
    STAMP(4);
    ldx(0); ldx(1); ldx(2); ldx(3);
    packq(6);
    ldx(4); ldx(5); ldx(6); ldx(7);
    packq(7); stash(8 + wv);
#pragma unroll
    for (int t = 0; t < 4; ++t) atomicAdd(&lds[LDS_CS / 4 + 4 * c + t], (unsigned)cs[t]);
    BAR();
    STAMP(5);

    v4i acc[4];
#pragma unroll
    for (int t = 0; t < 4; ++t) acc[t] = (v4i){0, 0, 0, 0};
    v4i bf[16][4];
    auto ldb = [&](int u) {
#pragma unroll
        for (int t = 0; t < 4; ++t) bf[u][t] = *(const v4i*)(ldsb + u * 4096 + t * 1024 + lane * 16);
    };
    auto mma = [&](int u) {
#pragma unroll
        for (int t = 0; t < 4; ++t) acc[t] = __builtin_amdgcn_mfma_i32_16x16x64_i8(xa[u], bf[u][t], acc[t], 0, 0, 0);
    };
    auto step = [&](int u, int pf) { if (pf >= 0) ldb(pf); FENCE(); mma(u); FENCE(); if (u + 8 < 16) ldx(u + 8); };
    ldb(0); FENCE();
    step(0, 1); step(1, 2); step(2, 3); step(3, 4); step(4, 5); step(5, 6); step(6, 7); step(7, 8);
    STAMP(6);
    step(8, 9); step(9, 10); step(10, 11); step(11, 12);
    v4i cst = *(const v4i*)(ldsb + LDS_CS + 16 * c);
    step(12, 13); step(13, 14); step(14, 15); step(15, -1);
    STAMP(7);

    const float sx = bf2f(sxh);
    const float zf = rnd2((v2f){(float)zx, 0.f}).x;
    auto ro = __builtin_amdgcn_make_buffer_rsrc((void*)out, 0, M_ * N_ * 2, 0x00020000);
    const unsigned ooff = ((m0 + 16 * wv + 4 * g) * N_ + n0 + 4 * c) * 2;
    v2f sc[2], of[2];
#pragma unroll
    for (int p = 0; p < 2; ++p) {
        v2f cf = rnd2((v2f){(float)cst[2 * p], (float)cst[2 * p + 1]});
        v2f zc = rnd2(cf * zf), sw, bi;
        sw.x = __builtin_bit_cast(float, swv[p] << 16); sw.y = __builtin_bit_cast(float, swv[p] & 0xffff0000u);
        bi.x = __builtin_bit_cast(float, biv[p] << 16); bi.y = __builtin_bit_cast(float, biv[p] & 0xffff0000u);
        sc[p] = sw * sx;
        of[p] = bi - zc * sc[p];
    }
#pragma unroll
    for (int r = 0; r < 4; ++r) {
        v2u o;
#pragma unroll
        for (int p = 0; p < 2; ++p) {
            v2f v = (v2f){(float)acc[2 * p][r], (float)acc[2 * p + 1][r]} * sc[p] + of[p];
            o[p] = __builtin_bit_cast(unsigned, __builtin_convertvector(v, v2bf));
        }
        __builtin_amdgcn_raw_buffer_store_b64(o, ro, ooff, r * N_ * 2, SC1);
    }
}

extern "C" void kernel_launch(void* const* d_in, const int* in_sizes, int n_in,
                              void* d_out, int out_size, void* d_ws, size_t ws_size,
                              hipStream_t stream) {
    w8a8_kernel<<<256, NT, 0, stream>>>((const int*)d_in[0], (const int*)d_in[1],
                                         (const unsigned short*)d_in[2], (const int*)d_in[3],
                                         (const unsigned short*)d_in[4], (const unsigned short*)d_in[5],
                                         (unsigned short*)d_out, (char*)d_ws);
}
```

```cpp
#include <hip/hip_runtime.h>
#include <hip/hip_bf16.h>
#include <stdint.h>

typedef int v4i __attribute__((ext_vector_type(4)));
typedef unsigned v4u __attribute__((ext_vector_type(4)));
typedef unsigned v2u __attribute__((ext_vector_type(2)));
typedef float v2f __attribute__((ext_vector_type(2)));
typedef float v4f __attribute__((ext_vector_type(4)));
typedef __bf16 v2bf __attribute__((ext_vector_type(2)));
typedef unsigned long long u64;
typedef __attribute__((address_space(1))) u64 gu64;
typedef __attribute__((address_space(3))) volatile unsigned lds_vu32;

constexpr int M_ = 512, K_ = 1024, N_ = 3584;
constexpr int BM = 128, BN = 64, NT = 12 * 64;
constexpr unsigned FLAG_STRIDE = 256, WS_FLAG = 4096, WS_ACK = 32768, WS_X8 = 262144;
constexpr u64 TAG_STEP = 0x9E3779B97F4A7C15ull;
constexpr int SC1 = 16;
constexpr unsigned SPIN_CAP = 1u << 15;
constexpr int LDS_CTL = 65536, LDS_PAR = LDS_CTL + 256, LDS_CS = LDS_PAR + 512, LDS_EPI = LDS_CS + 2048;
constexpr int LDS_BYTES = 84 * 1024;
enum { C_TAG_LO = 0, C_TAG_HI = 1, C_PAR = 2, C_OWN = 4, C_GROUP = 8, C_CSUM = 12, C_EPI = 13 };
constexpr int XD = 4;

#define FENCE() do { asm volatile("" ::: "memory"); __builtin_amdgcn_sched_barrier(0); } while (0)
#define VMWAIT(n) do { asm volatile("s_waitcnt vmcnt(%0)" :: "i"(n) : "memory"); __builtin_amdgcn_sched_barrier(0); } while (0)
#define LGKM0() do { asm volatile("s_waitcnt lgkmcnt(0)" ::: "memory"); __builtin_amdgcn_sched_barrier(0); } while (0)
#define BAR() do { asm volatile("s_waitcnt lgkmcnt(0)\n\ts_barrier" ::: "memory"); __builtin_amdgcn_sched_barrier(0); } while (0)

__device__ __forceinline__ unsigned pack4(unsigned a, unsigned b, unsigned c, unsigned d) {
    return __builtin_amdgcn_perm(b, a, 0x0c0c0400u) | __builtin_amdgcn_perm(d, c, 0x04000c0cu);
}
__device__ __forceinline__ float rnd_bf16(float v) {
    const v2bf b = __builtin_convertvector((v2f){v, 0.f}, v2bf);
    return __builtin_bit_cast(float, __builtin_bit_cast(unsigned, b) << 16);
}
__device__ __forceinline__ float bf2f(unsigned short h) { return __builtin_bit_cast(float, (unsigned)h << 16); }
__device__ __forceinline__ u64 ld_agent(const void* p) { return __hip_atomic_load((gu64*)p, __ATOMIC_RELAXED, __HIP_MEMORY_SCOPE_AGENT); }
__device__ __forceinline__ void st_agent(void* p, u64 v) { __hip_atomic_store((gu64*)p, v, __ATOMIC_RELAXED, __HIP_MEMORY_SCOPE_AGENT); }
__device__ __forceinline__ void lds_wait_ge(lds_vu32* w, unsigned v) {
#pragma nounroll
    for (unsigned spin = 0; spin < (SPIN_CAP << 4); ++spin) {
        if ((unsigned)__builtin_amdgcn_readfirstlane(*w) >= v) break;
        __builtin_amdgcn_s_sleep(1);
    }
    FENCE();
}
__device__ __forceinline__ void poll_flags(const char* p, u64 tag) {
#pragma nounroll
    for (unsigned spin = 0; spin < SPIN_CAP; ++spin) {
        if (__all(ld_agent(p) == tag)) break;
        __builtin_amdgcn_s_sleep(8);
    }
    FENCE();
}

__global__ __launch_bounds__(NT)
void w8a8_kernel(const int* __restrict__ qx, const int* __restrict__ qw,
                 const unsigned short* __restrict__ sxp, const int* __restrict__ zxp,
                 const unsigned short* __restrict__ swp, const unsigned short* __restrict__ biasp,
                 unsigned short* __restrict__ out, char* __restrict__ ws) {
    __shared__ __attribute__((aligned(16))) unsigned lds[LDS_BYTES / 4];
    const int lane = threadIdx.x & 63;
    const int wv = __builtin_amdgcn_readfirstlane(threadIdx.x >> 6);
    const int bid = blockIdx.x;
    const int grp = bid & 7, idx = bid >> 3;
    const int g = lane >> 4, c = lane & 15;
    char* ldsb = (char*)lds;
    lds_vu32* ctl = (lds_vu32*)(lds + LDS_CTL / 4);
    const int mt = idx & 3;
    const int n0 = (grp * 7 + ((idx - 4) >> 2)) * BN;
    const int pA = wv < 4 ? wv : wv + 4, pB = pA + 4;
    const int sA = (4 * mt + pA) & 15, sB = (4 * mt + pB) & 15;
    unsigned woff = (4 * g * N_ + n0 + 4 * c) * 4;
    unsigned wbA = (128 * (sA >> 1) + 16 * (sA & 1)) * N_ * 4;
    unsigned xoff = ((16 * bid + (lane >> 3)) * K_ + 128 * wv + 4 * (lane & 7)) * 4;
    int Lh = (idx - 4) * 37 + (lane < 37 ? lane : 36);
    Lh = Lh < 1024 ? Lh : 1023;
    unsigned hoff = ((Lh >> 8) * 128 + ((Lh >> 4) & 15)) * 4096 + (Lh & 15) * 128;
    asm volatile("" : "+v"(woff), "+s"(wbA), "+v"(xoff), "+v"(hoff));
    asm volatile("" :: "s"(qx), "s"(qw), "s"(sxp), "s"(zxp), "s"(swp), "s"(biasp), "s"(out), "s"(ws));

    if (idx < 4) {
        if (wv >= 8) return;
        auto rx = __builtin_amdgcn_make_buffer_rsrc((void*)qx, 0, M_ * K_ * 4, 0x00020000);
        const int r8 = lane >> 3, q8 = lane & 7;
        v4u xr[8];
#pragma unroll
        for (int n = 0; n < 8; ++n)
            xr[n] = __builtin_amdgcn_raw_buffer_load_b128(rx, xoff + (n & 3) * 128, (n >> 2) * 8 * 4096, 0);
        FENCE();
        u64 tag = 0;
        if (wv == 0) tag = ld_agent(ws) + TAG_STEP;
        auto rx8 = __builtin_amdgcn_make_buffer_rsrc((void*)(ws + WS_X8), 0, M_ * K_, 0x00020000);
        char* xt = ldsb + wv * 2048;
#pragma unroll
        for (int hh = 0; hh < 2; ++hh) {
            v4u p;
#pragma unroll
            for (int i = 0; i < 4; ++i) { const v4u v = xr[hh * 4 + i]; p[i] = pack4(v[0], v[1], v[2], v[3]); }
            *(v4u*)(xt + q8 * 256 + (((8 * hh + r8) ^ q8) * 16)) = p;
        }
#pragma unroll
        for (int s = 0; s < 2; ++s) {
            const int q = 4 * s + g;
            const v4u a = *(const v4u*)(xt + q * 256 + ((c ^ q) * 16));
            __builtin_amdgcn_raw_buffer_store_b128(a, rx8, ((bid * 16 + 2 * wv + s) * 64 + lane) * 16, 0, SC1);
        }
        VMWAIT(0);
        BAR();
        if (wv == 0) {
            if (lane == 0) { st_agent(ws + WS_FLAG + bid * FLAG_STRIDE, tag); st_agent(ws + WS_ACK + bid * 8, tag); }
            if (bid == 0) {
                __builtin_amdgcn_s_sleep(60);
#pragma nounroll
                for (unsigned spin = 0; spin < SPIN_CAP; ++spin) {
                    bool ok = true;
#pragma unroll
                    for (int k = 0; k < 4; ++k) ok &= (ld_agent(ws + WS_ACK + (k * 64 + lane) * 8) == tag);
                    if (__all(ok)) break;
                    __builtin_amdgcn_s_sleep(8);
                }
                if (lane == 0) st_agent(ws, tag);
            }
        }
        return;
    }

    if (wv < 8) {
        auto rw = __builtin_amdgcn_make_buffer_rsrc((void*)qw, 0, K_ * N_ * 4, 0x00020000);
        const unsigned wbB = (128 * (sB >> 1) + 16 * (sB & 1)) * N_ * 4;
        v4u wr[8][4], pk[4];
        int cs[4] = {0, 0, 0, 0};
        auto ld1 = [&](int n) {
            wr[n >> 2][n & 3] = __builtin_amdgcn_raw_buffer_load_b128(rw, woff, (n < 16 ? wbA : wbB) + (32 * ((n >> 2) & 3) + (n & 3)) * N_ * 4, 0);
            FENCE();
        };
        auto packq = [&](int i) {
#pragma unroll
            for (int t = 0; t < 4; ++t) {
                unsigned p = pack4(wr[i][0][t], wr[i][1][t], wr[i][2][t], wr[i][3][t]);
                cs[t] = __builtin_amdgcn_sdot4(p, 0x01010101, cs[t], false);
                asm volatile("" : "+v"(p), "+v"(cs[t]));
                pk[t][i & 3] = p;
            }
            FENCE();
        };
        auto stash = [&](int slot) {
#pragma unroll
            for (int t = 0; t < 4; ++t) *(v4u*)(ldsb + slot * 4096 + t * 1024 + lane * 16) = pk[t];
            LGKM0();
            if (lane == 0) atomicAdd(&lds[LDS_CTL / 4 + C_GROUP + (slot >> 2)], 1u);
            FENCE();
        };
        if (wv < 4) {
            unsigned xh = 0;
            if (wv == 0) {
                auto rx = __builtin_amdgcn_make_buffer_rsrc((void*)qx, 0, M_ * K_ * 4, 0x00020000);
                xh = __builtin_amdgcn_raw_buffer_load_b32(rx, hoff, grp * 65536 + 2048, 0);
                FENCE();
            }
#pragma unroll
            for (int n = 0; n < 8; ++n) ld1(n);
            BAR();
#pragma unroll
            for (int n = 8; n < 16; ++n) ld1(n);
            packq(0);
            VMWAIT(8);
            if (lane == 0) atomicAdd(&lds[LDS_CTL / 4 + C_OWN], 1u);
            packq(1); packq(2); packq(3);
            if (lane == 0) atomicAdd(&lds[LDS_CTL / 4 + C_OWN], 1u);
            stash(pA);
            asm volatile("" :: "v"(xh));
#pragma unroll
            for (int n = 16; n < 32; ++n) ld1(n);
        } else {
            BAR();
            lds_wait_ge(ctl + C_OWN, 4);
#pragma unroll
            for (int n = 0; n < 16; ++n) ld1(n);
#pragma unroll
            for (int n = 0; n < 16; ++n) {
                VMWAIT(15);
                ld1(n + 16);
                if ((n & 3) == 3) packq(n >> 2);
            }
            stash(pA);
        }
        packq(4); packq(5); packq(6); packq(7);
        stash(pB);
#pragma unroll
        for (int t = 0; t < 4; ++t) {
            auto r = __builtin_amdgcn_permlane32_swap(cs[t], cs[t], false, false);
            cs[t] = r[0] + r[1];
            r = __builtin_amdgcn_permlane16_swap(cs[t], cs[t], false, false);
            cs[t] = r[0] + r[1];
        }
        if (lane < 16) *(v4i*)(ldsb + LDS_CS + wv * 256 + c * 16) = (v4i){cs[0], cs[1], cs[2], cs[3]};
        LGKM0();
        unsigned n = 0;
        if (lane == 0) n = atomicAdd(&lds[LDS_CTL / 4 + C_CSUM], 1u);
        if (__builtin_amdgcn_readfirstlane(n) == 7) {
            FENCE();
            int cst = 0;
#pragma unroll
            for (int k = 0; k < 8; ++k) cst += *(const int*)(ldsb + LDS_CS + k * 256 + lane * 4);
            lds_wait_ge(ctl + C_PAR, 1);
            const float sw = bf2f(*(const unsigned short*)(ldsb + LDS_PAR + 2 * lane));
            const float bi = bf2f(*(const unsigned short*)(ldsb + LDS_PAR + 128 + 2 * lane));
            const float sx = bf2f((unsigned short)lds[LDS_PAR / 4 + 64]);
            const float zc = rnd_bf16(rnd_bf16((float)cst) * rnd_bf16((float)(int)lds[LDS_PAR / 4 + 65]));
            const float sc = sw * sx;
            *(float*)(ldsb + LDS_EPI + lane * 4) = sc;
            *(float*)(ldsb + LDS_EPI + 256 + lane * 4) = bi - zc * sc;
            LGKM0();
            ctl[C_EPI] = 1;
        }
        return;
    }

    const int mw = wv - 8;
    u64 tag;
    if (mw == 0) {
        tag = ld_agent(ws);
        const unsigned par = *(const unsigned*)((lane < 32 ? swp : biasp) + n0 + 2 * (lane & 31));
        const unsigned psx = __builtin_nontemporal_load(sxp);
        const unsigned pzx = __builtin_nontemporal_load(zxp);
        FENCE();
        lds[LDS_CTL / 4 + lane] = 0;
        BAR();
        tag += TAG_STEP;
        lds[LDS_PAR / 4 + lane] = par;
        lds[LDS_PAR / 4 + 64] = psx;
        lds[LDS_PAR / 4 + 65] = pzx;
        FENCE();
        ctl[C_TAG_LO] = (unsigned)tag; ctl[C_TAG_HI] = (unsigned)(tag >> 32); ctl[C_PAR] = 1;
        if (lane == 0) st_agent(ws + WS_ACK + bid * 8, tag);
    } else {
        BAR();
        lds_wait_ge(ctl + C_PAR, 1);
        tag = ((u64)ctl[C_TAG_HI] << 32) | ctl[C_TAG_LO];
    }
    auto rx8 = __builtin_amdgcn_make_buffer_rsrc((void*)(ws + WS_X8), 0, M_ * K_, 0x00020000);
    const unsigned x8off = (mt * 8 + 2 * mw) * 16384 + lane * 16;
    v4i xa[16][2], bf[16][4], acc[2][4];
    auto ldx = [&](int u, int m) {
        xa[u][m] = __builtin_bit_cast(v4i, __builtin_amdgcn_raw_buffer_load_b128(rx8, x8off, ((4 * mt + u) & 15) * 1024 + m * 16384, SC1));
        FENCE();
    };
    auto ldb = [&](int u, int t) { bf[u][t] = *(const v4i*)(ldsb + u * 4096 + t * 1024 + lane * 16); FENCE(); };
#pragma unroll
    for (int t = 0; t < 8; ++t) acc[t >> 2][t & 3] = (v4i){0, 0, 0, 0};
    lds_wait_ge(ctl + C_OWN, 8);
    poll_flags(ws + WS_FLAG + (mt * 8 + 2 * mw + (lane & 1)) * FLAG_STRIDE, tag);
#pragma unroll
    for (int p = 0; p < 2 * XD; ++p) ldx(p >> 1, p & 1);
    v4f scv, ofv;
    unsigned rdy = 0;
    lds_wait_ge(ctl + C_GROUP, 4);
#pragma unroll
    for (int t = 0; t < 4; ++t) ldb(0, t);
#pragma unroll
    for (int u = 0; u < 16; ++u) {
#pragma unroll
        for (int k = 0; k < 8; ++k) {
            acc[k >> 2][k & 3] = __builtin_amdgcn_mfma_i32_16x16x64_i8(xa[u][k >> 2], bf[u][k & 3], acc[k >> 2][k & 3], 0, 0, 0);
            FENCE();
            if (k == 0 && u + 1 < 16 && ((u + 1) & 3) == 0) {
                if ((unsigned)__builtin_amdgcn_readfirstlane(rdy) < 4) lds_wait_ge(ctl + C_GROUP + ((u + 1) >> 2), 4);
                FENCE();
            }
            if (k < 4 && u + 1 < 16) ldb(u + 1, k);
            if ((k == 4 || k == 5) && u + XD < 16) ldx(u + XD, k - 4);
            if (k == 6 && u + 2 < 16 && ((u + 2) & 3) == 0) { rdy = ctl[C_GROUP + ((u + 2) >> 2)]; FENCE(); }
            if (k == 6 && u == 13) { rdy = ctl[C_EPI]; FENCE(); }
            if (k == 4 && u == 14) {
                if (__builtin_amdgcn_readfirstlane(rdy) == 0) lds_wait_ge(ctl + C_EPI, 1);
                FENCE();
                scv = *(const v4f*)(ldsb + LDS_EPI + 16 * c);
                ofv = *(const v4f*)(ldsb + LDS_EPI + 256 + 16 * c);
                FENCE();
            }
        }
    }
    auto ro = __builtin_amdgcn_make_buffer_rsrc((void*)out, 0, M_ * N_ * 2, 0x00020000);
    const unsigned ooff = ((mt * BM + 32 * mw + 4 * g) * N_ + n0 + 4 * c) * 2;
#pragma unroll
    for (int m = 0; m < 2; ++m)
#pragma unroll
        for (int r = 0; r < 4; ++r) {
            v2u o;
#pragma unroll
            for (int p = 0; p < 2; ++p) {
                const v2f v = (v2f){(float)acc[m][2 * p][r], (float)acc[m][2 * p + 1][r]} * (v2f){scv[2 * p], scv[2 * p + 1]}
                              + (v2f){ofv[2 * p], ofv[2 * p + 1]};
                o[p] = __builtin_bit_cast(unsigned, __builtin_convertvector(v, v2bf));
            }
            __builtin_amdgcn_raw_buffer_store_b64(o, ro, ooff, (16 * m + r) * N_ * 2, SC1);
        }
}

extern "C" void kernel_launch(void* const* d_in, const int* in_sizes, int n_in,
                              void* d_out, int out_size, void* d_ws, size_t ws_size,
                              hipStream_t stream) {
    w8a8_kernel<<<256, NT, 0, stream>>>((const int*)d_in[0], (const int*)d_in[1],
                                         (const unsigned short*)d_in[2], (const int*)d_in[3],
                                         (const unsigned short*)d_in[4], (const unsigned short*)d_in[5],
                                         (unsigned short*)d_out, (char*)d_ws);
}
```

```cpp
#include <hip/hip_runtime.h>
#include <hip/hip_bf16.h>
#include <stdint.h>

typedef int v4i __attribute__((ext_vector_type(4)));
typedef unsigned v4u __attribute__((ext_vector_type(4)));
typedef unsigned v2u __attribute__((ext_vector_type(2)));
typedef float v2f __attribute__((ext_vector_type(2)));
typedef float v4f __attribute__((ext_vector_type(4)));
typedef __bf16 v2bf __attribute__((ext_vector_type(2)));
typedef unsigned long long u64;
typedef __attribute__((address_space(1))) u64 gu64;
typedef __attribute__((address_space(3))) volatile unsigned lds_vu32;

constexpr int M_ = 512, K_ = 1024, N_ = 3584;
constexpr int BM = 128, BN = 64, NT = 12 * 64;
constexpr unsigned FLAG_STRIDE = 256, WS_FLAG = 4096, WS_ACK = 32768, WS_X8 = 262144;
constexpr u64 TAG_STEP = 0x9E3779B97F4A7C15ull;
constexpr int SC1 = 16;
constexpr unsigned SPIN_CAP = 1u << 15;
constexpr int LDS_CTL = 65536, LDS_PAR = LDS_CTL + 256, LDS_CS = LDS_PAR + 512, LDS_EPI = LDS_CS + 2048;
constexpr int LDS_BYTES = 84 * 1024;
enum { C_TAG_LO = 0, C_TAG_HI = 1, C_PAR = 2, C_OWN = 4, C_GROUP = 8, C_CSUM = 12, C_EPI = 13 };
constexpr int XD = 4;

#define VA 1
#define VC 1
#define HPOS 4
#define FENCE() do { asm volatile("" ::: "memory"); __builtin_amdgcn_sched_barrier(0); } while (0)
#define VMWAIT(n) do { asm volatile("s_waitcnt vmcnt(%0)" :: "i"(n) : "memory"); __builtin_amdgcn_sched_barrier(0); } while (0)
#define LGKM0() do { asm volatile("s_waitcnt lgkmcnt(0)" ::: "memory"); __builtin_amdgcn_sched_barrier(0); } while (0)
#define BAR() do { asm volatile("s_waitcnt lgkmcnt(0)\n\ts_barrier" ::: "memory"); __builtin_amdgcn_sched_barrier(0); } while (0)

__device__ __forceinline__ unsigned pack4(unsigned a, unsigned b, unsigned c, unsigned d) {
    return __builtin_amdgcn_perm(b, a, 0x0c0c0400u) | __builtin_amdgcn_perm(d, c, 0x04000c0cu);
}
__device__ __forceinline__ float rnd_bf16(float v) {
    const v2bf b = __builtin_convertvector((v2f){v, 0.f}, v2bf);
    return __builtin_bit_cast(float, __builtin_bit_cast(unsigned, b) << 16);
}
__device__ __forceinline__ float bf2f(unsigned short h) { return __builtin_bit_cast(float, (unsigned)h << 16); }
__device__ __forceinline__ u64 ld_agent(const void* p) { return __hip_atomic_load((gu64*)p, __ATOMIC_RELAXED, __HIP_MEMORY_SCOPE_AGENT); }
__device__ __forceinline__ void st_agent(void* p, u64 v) { __hip_atomic_store((gu64*)p, v, __ATOMIC_RELAXED, __HIP_MEMORY_SCOPE_AGENT); }
__device__ __forceinline__ void lds_wait_ge(lds_vu32* w, unsigned v) {
#pragma nounroll
    for (unsigned spin = 0; spin < (SPIN_CAP << 4); ++spin) {
        if ((unsigned)__builtin_amdgcn_readfirstlane(*w) >= v) break;
        __builtin_amdgcn_s_sleep(1);
    }
    FENCE();
}
__device__ __forceinline__ void poll_flags(const char* p, u64 tag) {
#pragma nounroll
    for (unsigned spin = 0; spin < SPIN_CAP; ++spin) {
        if (__all(ld_agent(p) == tag)) break;
        __builtin_amdgcn_s_sleep(8);
    }
    FENCE();
}

struct Loader {
    v4u wr[8][4], pk[4];
    int cs[4];
    __device__ __forceinline__ void packq(int i) {
#pragma unroll
        for (int t = 0; t < 4; ++t) {
            unsigned p = pack4(wr[i][0][t], wr[i][1][t], wr[i][2][t], wr[i][3][t]);
            cs[t] = __builtin_amdgcn_sdot4(p, 0x01010101, cs[t], false);
            asm volatile("" : "+v"(p), "+v"(cs[t]));
            pk[t][i & 3] = p;
        }
        FENCE();
    }
    __device__ __forceinline__ void stash(unsigned* lds, int slot, int lane) {
#pragma unroll
        for (int t = 0; t < 4; ++t) *(v4u*)((char*)lds + slot * 4096 + t * 1024 + lane * 16) = pk[t];
        LGKM0();
        if (lane == 0) atomicAdd(&lds[LDS_CTL / 4 + C_GROUP + (slot >> 2)], 1u);
        FENCE();
    }
    __device__ __forceinline__ void finish(unsigned* lds, int slot, int wv, int lane) {
        char* ldsb = (char*)lds;
        lds_vu32* ctl = (lds_vu32*)(lds + LDS_CTL / 4);
        packq(4); packq(5); packq(6); packq(7);
        stash(lds, slot, lane);
#pragma unroll
        for (int t = 0; t < 4; ++t) {
            auto r = __builtin_amdgcn_permlane32_swap(cs[t], cs[t], false, false);
            cs[t] = r[0] + r[1];
            r = __builtin_amdgcn_permlane16_swap(cs[t], cs[t], false, false);
            cs[t] = r[0] + r[1];
        }
        if (lane < 16) *(v4i*)(ldsb + LDS_CS + wv * 256 + lane * 16) = (v4i){cs[0], cs[1], cs[2], cs[3]};
        LGKM0();
        unsigned n = 0;
        if (lane == 0) n = atomicAdd(&lds[LDS_CTL / 4 + C_CSUM], 1u);
        if (__builtin_amdgcn_readfirstlane(n) == 7) {
            FENCE();
            int cst = 0;
#pragma unroll
            for (int k = 0; k < 8; ++k) cst += *(const int*)(ldsb + LDS_CS + k * 256 + lane * 4);
            lds_wait_ge(ctl + C_PAR, 1);
            const float sw = bf2f(*(const unsigned short*)(ldsb + LDS_PAR + 2 * lane));
            const float bi = bf2f(*(const unsigned short*)(ldsb + LDS_PAR + 128 + 2 * lane));
            const float sx = bf2f((unsigned short)lds[LDS_PAR / 4 + 64]);
            const float zc = rnd_bf16(rnd_bf16((float)cst) * rnd_bf16((float)(int)lds[LDS_PAR / 4 + 65]));
            const float sc = sw * sx;
            *(float*)(ldsb + LDS_EPI + lane * 4) = sc;
            *(float*)(ldsb + LDS_EPI + 256 + lane * 4) = bi - zc * sc;
            LGKM0();
            ctl[C_EPI] = 1;
        }
    }
};

__global__ __launch_bounds__(NT)
void w8a8_kernel(const int* __restrict__ qx, const int* __restrict__ qw,
                 const unsigned short* __restrict__ sxp, const int* __restrict__ zxp,
                 const unsigned short* __restrict__ swp, const unsigned short* __restrict__ biasp,
                 unsigned short* __restrict__ out, char* __restrict__ ws) {
    __shared__ __attribute__((aligned(16))) unsigned lds[LDS_BYTES / 4];
    const int lane = threadIdx.x & 63;
    const int wv = __builtin_amdgcn_readfirstlane(threadIdx.x >> 6);
    const int bid = blockIdx.x;
    const int grp = bid & 7, idx = bid >> 3;
    const int g = lane >> 4, c = lane & 15;
    char* ldsb = (char*)lds;
    lds_vu32* ctl = (lds_vu32*)(lds + LDS_CTL / 4);
    const bool conv = idx < 4;
    const int mt = idx & 3;
    const int n0 = (grp * 7 + ((idx - 4) >> 2)) * BN;
    auto kstep_off = [&](int slot) { const int s = (4 * mt + slot) & 15; return (unsigned)((128 * (s >> 1) + 16 * (s & 1)) * (N_ * 4) + n0 * 4); };

    {
        unsigned soX = bid * (16 * K_ * 4) + wv * 512;
        unsigned soW = (256 * mt + 128 * (wv >> 1) + 16 * (wv & 1)) * (N_ * 4) + n0 * 4;
        unsigned soH = grp * 65536 + 2048 + (idx - 4) * 128;
        asm volatile("" : "+s"(soX), "+s"(soW), "+s"(soH));
        const unsigned sh = conv ? 3 : 4, rowb = conv ? K_ * 4 : 4 * N_ * 4;
        unsigned voff = __umul24(lane >> sh, rowb) + (lane & ((1u << sh) - 1)) * 16;
        unsigned so0 = conv ? soX : soW;
        unsigned stA = conv ? 128 : N_ * 4, stB = conv ? 8 * K_ * 4 : 32 * N_ * 4;
        unsigned nb0 = conv ? M_ * K_ * 4 : K_ * N_ * 4;
        int Lh = (idx - 4) * 37 + (lane < 37 ? lane : 36);
        Lh = Lh < 1024 ? Lh : 1023;
        unsigned hoff = ((Lh >> 8) * 128 + ((Lh >> 4) & 15)) * 4096 + (Lh & 15) * 128;
        soH = (wv == 0 && !conv) ? grp * 65536 + 2048 : 0x40000000u;
        asm volatile("" : "+s"(so0), "+s"(stA), "+s"(stB), "+s"(nb0), "+s"(soH));
        asm volatile("" : "+v"(voff), "+v"(hoff));
        asm volatile("" :: "s"(qx), "s"(qw), "s"(sxp), "s"(zxp), "s"(swp), "s"(biasp), "s"(out), "s"(ws));
        if (__builtin_expect(wv < (conv ? 8 : 4), 1)) {
            auto r0 = __builtin_amdgcn_make_buffer_rsrc((void*)(conv ? qx : qw), 0, nb0, 0x00020000);
            Loader L;
            auto rx = __builtin_amdgcn_make_buffer_rsrc((void*)qx, 0, M_ * K_ * 4, 0x00020000);
            unsigned xh = 0;
            if (HPOS == 0) { xh = __builtin_amdgcn_raw_buffer_load_b32(rx, hoff, soH, 0); FENCE(); }
#pragma unroll
            for (int n = 0; n < 8; ++n) {
                L.wr[n >> 2][n & 3] = __builtin_amdgcn_raw_buffer_load_b128(r0, voff, so0 + (n & 3) * stA + (n >> 2) * stB, 0);
                FENCE();
                if (HPOS == n + 1) { xh = __builtin_amdgcn_raw_buffer_load_b32(rx, hoff, soH, 0); FENCE(); }
            }
            if (conv) {
                const int r8 = lane >> 3, q8 = lane & 7;
                auto rx8 = __builtin_amdgcn_make_buffer_rsrc((void*)(ws + WS_X8), 0, M_ * K_, 0x00020000);
                char* xt = ldsb + wv * 2048;
#pragma unroll
                for (int hh = 0; hh < 2; ++hh) {
                    v4u p;
#pragma unroll
                    for (int i = 0; i < 4; ++i) { const v4u v = L.wr[hh][i]; p[i] = pack4(v[0], v[1], v[2], v[3]); }
                    *(v4u*)(xt + q8 * 256 + (((8 * hh + r8) ^ q8) * 16)) = p;
                }
#pragma unroll
                for (int s = 0; s < 2; ++s) {
                    const int q = 4 * s + g;
                    const v4u a = *(const v4u*)(xt + q * 256 + ((c ^ q) * 16));
                    __builtin_amdgcn_raw_buffer_store_b128(a, rx8, ((bid * 16 + 2 * wv + s) * 64 + lane) * 16, 0, SC1);
                }
                asm volatile("" :: "v"(xh));
                VMWAIT(0);
                BAR();
                return;
            }
            BAR();
            auto rw = __builtin_amdgcn_make_buffer_rsrc((void*)qw, 0, K_ * N_ * 4, 0x00020000);
            const unsigned soA = kstep_off(wv);
#pragma unroll
            for (int n = 8; n < 16; ++n) {
                L.wr[n >> 2][n & 3] = __builtin_amdgcn_raw_buffer_load_b128(rw, voff, soA + (32 * (n >> 2) + (n & 3)) * (N_ * 4), 0);
                FENCE();
            }
            L.cs[0] = L.cs[1] = L.cs[2] = L.cs[3] = 0;
            L.packq(0);
            VMWAIT(8);
            if (lane == 0) atomicAdd(&lds[LDS_CTL / 4 + C_OWN], 1u);
            L.packq(1); L.packq(2); L.packq(3);
            if (lane == 0) atomicAdd(&lds[LDS_CTL / 4 + C_OWN], 1u);
            L.stash(lds, wv, lane);
            asm volatile("" :: "v"(xh));
            const unsigned soB = kstep_off(wv + 4);
#pragma unroll
            for (int n = 0; n < 16; ++n) {
                L.wr[4 + (n >> 2)][n & 3] = __builtin_amdgcn_raw_buffer_load_b128(rw, voff, soB + (32 * (n >> 2) + (n & 3)) * (N_ * 4), 0);
                FENCE();
            }
            L.finish(lds, wv + 4, wv, lane);
            return;
        }
        if (conv) {
            if (wv != 8) return;
            const u64 tag = ld_agent(ws) + TAG_STEP;
            BAR();
            if (lane == 0) { st_agent(ws + WS_FLAG + bid * FLAG_STRIDE, tag); st_agent(ws + WS_ACK + bid * 8, tag); }
            if (bid == 0) {
                __builtin_amdgcn_s_sleep(60);
#pragma nounroll
                for (unsigned spin = 0; spin < SPIN_CAP; ++spin) {
                    bool ok = true;
#pragma unroll
                    for (int k = 0; k < 4; ++k) ok &= (ld_agent(ws + WS_ACK + (k * 64 + lane) * 8) == tag);
                    if (__all(ok)) break;
                    __builtin_amdgcn_s_sleep(8);
                }
                if (lane == 0) st_agent(ws, tag);
            }
            return;
        }
        if (wv < 8) {
            BAR();
            auto rw = __builtin_amdgcn_make_buffer_rsrc((void*)qw, 0, K_ * N_ * 4, 0x00020000);
            const unsigned soA = kstep_off(wv + 4), soB = kstep_off(wv + 8);
            Loader L;
            L.cs[0] = L.cs[1] = L.cs[2] = L.cs[3] = 0;
            auto ld1 = [&](int n) {
                L.wr[n >> 2][n & 3] = __builtin_amdgcn_raw_buffer_load_b128(rw, voff, (n < 16 ? soA : soB) + (32 * ((n >> 2) & 3) + (n & 3)) * (N_ * 4), 0);
                FENCE();
            };
            lds_wait_ge(ctl + C_OWN, 4);
#pragma unroll
            for (int n = 0; n < 16; ++n) ld1(n);
#pragma unroll
            for (int n = 0; n < 16; ++n) {
                VMWAIT(15);
                ld1(n + 16);
                if ((n & 3) == 3) L.packq(n >> 2);
            }
            L.stash(lds, wv + 4, lane);
            L.finish(lds, wv + 8, wv, lane);
            return;
        }
    }

    const int mw = wv - 8;
    u64 tag;
    if (mw == 0) {
        tag = ld_agent(ws);
        const unsigned par = *(const unsigned*)((lane < 32 ? swp : biasp) + n0 + 2 * (lane & 31));
        const unsigned psx = __builtin_nontemporal_load(sxp);
        const unsigned pzx = __builtin_nontemporal_load(zxp);
        FENCE();
        lds[LDS_CTL / 4 + lane] = 0;
        BAR();
        tag += TAG_STEP;
        lds[LDS_PAR / 4 + lane] = par;
        lds[LDS_PAR / 4 + 64] = psx;
        lds[LDS_PAR / 4 + 65] = pzx;
        FENCE();
        ctl[C_TAG_LO] = (unsigned)tag; ctl[C_TAG_HI] = (unsigned)(tag >> 32); ctl[C_PAR] = 1;
        if (lane == 0) st_agent(ws + WS_ACK + bid * 8, tag);
    } else {
        BAR();
        lds_wait_ge(ctl + C_PAR, 1);
        tag = ((u64)ctl[C_TAG_HI] << 32) | ctl[C_TAG_LO];
    }
    auto rx8 = __builtin_amdgcn_make_buffer_rsrc((void*)(ws + WS_X8), 0, M_ * K_, 0x00020000);
    const unsigned x8off = (mt * 8 + 2 * mw) * 16384 + lane * 16;
    v4i xa[16][2], bf[16][4], acc[2][4];
    auto ldx = [&](int u, int m) {
        xa[u][m] = __builtin_bit_cast(v4i, __builtin_amdgcn_raw_buffer_load_b128(rx8, x8off, ((4 * mt + u) & 15) * 1024 + m * 16384, SC1));
        FENCE();
    };
    auto ldb = [&](int u, int t) { bf[u][t] = *(const v4i*)(ldsb + u * 4096 + t * 1024 + lane * 16); FENCE(); };
#pragma unroll
    for (int t = 0; t < 8; ++t) acc[t >> 2][t & 3] = (v4i){0, 0, 0, 0};
    lds_wait_ge(ctl + C_OWN, 8);
    poll_flags(ws + WS_FLAG + (mt * 8 + 2 * mw + (lane & 1)) * FLAG_STRIDE, tag);
#pragma unroll
    for (int p = 0; p < 2 * XD; ++p) ldx(p >> 1, p & 1);
    v4f scv, ofv;
    unsigned rdy = 0;
    lds_wait_ge(ctl + C_GROUP, 4);
#pragma unroll
    for (int t = 0; t < 4; ++t) ldb(0, t);
#pragma unroll
    for (int u = 0; u < 16; ++u) {
#pragma unroll
        for (int k = 0; k < 8; ++k) {
            acc[k >> 2][k & 3] = __builtin_amdgcn_mfma_i32_16x16x64_i8(xa[u][k >> 2], bf[u][k & 3], acc[k >> 2][k & 3], 0, 0, 0);
            FENCE();
            if (k == 0 && u + 1 < 16 && ((u + 1) & 3) == 0) {
                if ((unsigned)__builtin_amdgcn_readfirstlane(rdy) < 4) lds_wait_ge(ctl + C_GROUP + ((u + 1) >> 2), 4);
                FENCE();
            }
            if (k < 4 && u + 1 < 16) ldb(u + 1, k);
            if ((k == 4 || k == 5) && u + XD < 16) ldx(u + XD, k - 4);
            if (k == 6 && u + 2 < 16 && ((u + 2) & 3) == 0) { rdy = ctl[C_GROUP + ((u + 2) >> 2)]; FENCE(); }
            if (k == 6 && u == 13) { rdy = ctl[C_EPI]; FENCE(); }
            if (k == 4 && u == 14) {
                if (__builtin_amdgcn_readfirstlane(rdy) == 0) lds_wait_ge(ctl + C_EPI, 1);
                FENCE();
                scv = *(const v4f*)(ldsb + LDS_EPI + 16 * c);
                ofv = *(const v4f*)(ldsb + LDS_EPI + 256 + 16 * c);
                FENCE();
            }
        }
    }
    auto ro = __builtin_amdgcn_make_buffer_rsrc((void*)out, 0, M_ * N_ * 2, 0x00020000);
    const unsigned ooff = ((mt * BM + 32 * mw + 4 * g) * N_ + n0 + 4 * c) * 2;
#pragma unroll
    for (int m = 0; m < 2; ++m)
#pragma unroll
        for (int r = 0; r < 4; ++r) {
            v2u o;
#pragma unroll
            for (int p = 0; p < 2; ++p) {
                const v2f v = (v2f){(float)acc[m][2 * p][r], (float)acc[m][2 * p + 1][r]} * (v2f){scv[2 * p], scv[2 * p + 1]}
                              + (v2f){ofv[2 * p], ofv[2 * p + 1]};
                o[p] = __builtin_bit_cast(unsigned, __builtin_convertvector(v, v2bf));
            }
            __builtin_amdgcn_raw_buffer_store_b64(o, ro, ooff, (16 * m + r) * N_ * 2, SC1);
        }
}

extern "C" void kernel_launch(void* const* d_in, const int* in_sizes, int n_in,
                              void* d_out, int out_size, void* d_ws, size_t ws_size,
                              hipStream_t stream) {
    w8a8_kernel<<<256, NT, 0, stream>>>((const int*)d_in[0], (const int*)d_in[1],
                                         (const unsigned short*)d_in[2], (const int*)d_in[3],
                                         (const unsigned short*)d_in[4], (const unsigned short*)d_in[5],
                                         (unsigned short*)d_out, (char*)d_ws);
}
```

```cpp
#include <hip/hip_runtime.h>
#include <hip/hip_bf16.h>
#include <stdint.h>

typedef int v4i __attribute__((ext_vector_type(4)));
typedef unsigned v4u __attribute__((ext_vector_type(4)));
typedef unsigned v2u __attribute__((ext_vector_type(2)));
typedef float v2f __attribute__((ext_vector_type(2)));
typedef float v4f __attribute__((ext_vector_type(4)));
typedef __bf16 v2bf __attribute__((ext_vector_type(2)));
typedef unsigned long long u64;
typedef __attribute__((address_space(1))) u64 gu64;
typedef __attribute__((address_space(3))) volatile unsigned lds_vu32;

constexpr int M_ = 512, K_ = 1024, N_ = 3584;
constexpr int BM = 128, BN = 64, NT = 12 * 64;
constexpr unsigned FLAG_STRIDE = 256, WS_FLAG = 4096, WS_ACK = 32768, WS_X8 = 262144;
constexpr u64 TAG_STEP = 0x9E3779B97F4A7C15ull;
constexpr int SC1 = 16;
constexpr unsigned SPIN_CAP = 1u << 15;
constexpr int LDS_CTL = 65536, LDS_PAR = LDS_CTL + 256, LDS_CS = LDS_PAR + 512, LDS_EPI = LDS_CS + 2048;
constexpr int LDS_BYTES = 84 * 1024;
enum { C_TAG_LO = 0, C_TAG_HI = 1, C_PAR = 2, C_OWN = 4, C_GROUP = 8, C_CSUM = 12, C_EPI = 13 };
constexpr unsigned CODE_HINT_BYTES = 10240;
constexpr int XD = 4;

#define PIN_ARGS() asm volatile("" :: "s"(qx), "s"(qw), "s"(sxp), "s"(zxp), "s"(swp), "s"(biasp), "s"(out), "s"(ws))
#define FENCE() do { asm volatile("" ::: "memory"); __builtin_amdgcn_sched_barrier(0); } while (0)
#define VMWAIT(n) do { asm volatile("s_waitcnt vmcnt(%0)" :: "i"(n) : "memory"); __builtin_amdgcn_sched_barrier(0); } while (0)
#define LGKM0() do { asm volatile("s_waitcnt lgkmcnt(0)" ::: "memory"); __builtin_amdgcn_sched_barrier(0); } while (0)
#define BAR() do { asm volatile("s_waitcnt lgkmcnt(0)\n\ts_barrier" ::: "memory"); __builtin_amdgcn_sched_barrier(0); } while (0)

__device__ __forceinline__ unsigned pack4(unsigned a, unsigned b, unsigned c, unsigned d) {
    return __builtin_amdgcn_perm(b, a, 0x0c0c0400u) | __builtin_amdgcn_perm(d, c, 0x04000c0cu);
}
__device__ __forceinline__ float rnd_bf16(float v) {
    const v2bf b = __builtin_convertvector((v2f){v, 0.f}, v2bf);
    return __builtin_bit_cast(float, __builtin_bit_cast(unsigned, b) << 16);
}
__device__ __forceinline__ float bf2f(unsigned short h) { return __builtin_bit_cast(float, (unsigned)h << 16); }
__device__ __forceinline__ u64 ld_agent(const void* p) { return __hip_atomic_load((gu64*)p, __ATOMIC_RELAXED, __HIP_MEMORY_SCOPE_AGENT); }
__device__ __forceinline__ void st_agent(void* p, u64 v) { __hip_atomic_store((gu64*)p, v, __ATOMIC_RELAXED, __HIP_MEMORY_SCOPE_AGENT); }
__device__ __forceinline__ void lds_wait_ge(lds_vu32* w, unsigned v) {
#pragma nounroll
    for (unsigned spin = 0; spin < (SPIN_CAP << 4); ++spin) {
        if ((unsigned)__builtin_amdgcn_readfirstlane(*w) >= v) break;
        __builtin_amdgcn_s_sleep(1);
    }
    FENCE();
}
__device__ __forceinline__ void poll_flags(const char* p, u64 tag) {
#pragma nounroll
    for (unsigned spin = 0; spin < SPIN_CAP; ++spin) {
        if (__all(ld_agent(p) == tag)) break;
        __builtin_amdgcn_s_sleep(8);
    }
    FENCE();
}

struct Loader {
    v4u wr[8][4], pk[4];
    int cs[4];
    __device__ __forceinline__ void packq(int i) {
#pragma unroll
        for (int t = 0; t < 4; ++t) {
            unsigned p = pack4(wr[i][0][t], wr[i][1][t], wr[i][2][t], wr[i][3][t]);
            cs[t] = __builtin_amdgcn_sdot4(p, 0x01010101, cs[t], false);
            asm volatile("" : "+v"(p), "+v"(cs[t]));
            pk[t][i & 3] = p;
        }
        FENCE();
    }
    __device__ __forceinline__ void stash(unsigned* lds, int slot, int lane) {
#pragma unroll
        for (int t = 0; t < 4; ++t) *(v4u*)((char*)lds + slot * 4096 + t * 1024 + lane * 16) = pk[t];
        LGKM0();
        if (lane == 0) atomicAdd(&lds[LDS_CTL / 4 + C_GROUP + (slot >> 2)], 1u);
        FENCE();
    }
    __device__ __forceinline__ void finish(unsigned* lds, int slot, int wv, int lane) {
        char* ldsb = (char*)lds;
        lds_vu32* ctl = (lds_vu32*)(lds + LDS_CTL / 4);
        packq(4); packq(5); packq(6); packq(7);
        stash(lds, slot, lane);
#pragma unroll
        for (int t = 0; t < 4; ++t) {
            auto r = __builtin_amdgcn_permlane32_swap(cs[t], cs[t], false, false);
            cs[t] = r[0] + r[1];
            r = __builtin_amdgcn_permlane16_swap(cs[t], cs[t], false, false);
            cs[t] = r[0] + r[1];
        }
        if (lane < 16) *(v4i*)(ldsb + LDS_CS + wv * 256 + lane * 16) = (v4i){cs[0], cs[1], cs[2], cs[3]};
        LGKM0();
        unsigned n = 0;
        if (lane == 0) n = atomicAdd(&lds[LDS_CTL / 4 + C_CSUM], 1u);
        if (__builtin_amdgcn_readfirstlane(n) == 7) {
            FENCE();
            int cst = 0;
#pragma unroll
            for (int k = 0; k < 8; ++k) cst += *(const int*)(ldsb + LDS_CS + k * 256 + lane * 4);
            lds_wait_ge(ctl + C_PAR, 1);
            const float sw = bf2f(*(const unsigned short*)(ldsb + LDS_PAR + 2 * lane));
            const float bi = bf2f(*(const unsigned short*)(ldsb + LDS_PAR + 128 + 2 * lane));
            const float sx = bf2f((unsigned short)lds[LDS_PAR / 4 + 64]);
            const float zc = rnd_bf16(rnd_bf16((float)cst) * rnd_bf16((float)(int)lds[LDS_PAR / 4 + 65]));
            const float sc = sw * sx;
            *(float*)(ldsb + LDS_EPI + lane * 4) = sc;
            *(float*)(ldsb + LDS_EPI + 256 + lane * 4) = bi - zc * sc;
            LGKM0();
            ctl[C_EPI] = 1;
        }
    }
};

__global__ __launch_bounds__(NT) __attribute__((aligned(4096)))
void w8a8_kernel(const int* __restrict__ qx, const int* __restrict__ qw,
                 const unsigned short* __restrict__ sxp, const int* __restrict__ zxp,
                 const unsigned short* __restrict__ swp, const unsigned short* __restrict__ biasp,
                 unsigned short* __restrict__ out, char* __restrict__ ws) {
    __shared__ __attribute__((aligned(16))) unsigned lds[LDS_BYTES / 4];
    u64 pc0;
    asm volatile("s_getpc_b64 %0" : "=s"(pc0));
    const int lane = threadIdx.x & 63;
    const int wv = __builtin_amdgcn_readfirstlane(threadIdx.x >> 6);
    const int grp = blockIdx.x, mt = blockIdx.y, bz = blockIdx.z;
    const int idx = bz * 4 + mt, bid = idx * 8 + grp;
    const int g = lane >> 4, c = lane & 15;
    char* ldsb = (char*)lds;
    lds_vu32* ctl = (lds_vu32*)(lds + LDS_CTL / 4);
    const bool conv = bz == 0;
    const int n0 = (grp * 7 + bz - 1) * BN;
    auto kstep_off = [&](int slot) { const int s = (4 * mt + slot) & 15; return (unsigned)((128 * (s >> 1) + 16 * (s & 1)) * (N_ * 4) + n0 * 4); };

    {
        if (__builtin_expect(wv < (conv ? 8 : 4), 1)) {
            unsigned soX = (mt * 8 + grp) * (16 * K_ * 4) + wv * 512;
            unsigned soW = (256 * mt + 128 * (wv >> 1) + 16 * (wv & 1)) * (N_ * 4) + n0 * 4;
            asm volatile("" : "+s"(soX), "+s"(soW));
            const unsigned sh = conv ? 3 : 4, rowb = conv ? K_ * 4 : 4 * N_ * 4;
            unsigned voff = __umul24(lane >> sh, rowb) + (lane & ((1u << sh) - 1)) * 16;
            unsigned so0 = conv ? soX : soW;
            unsigned stA = conv ? 128 : N_ * 4, stB = conv ? 8 * K_ * 4 : 32 * N_ * 4;
            unsigned nb0 = conv ? M_ * K_ * 4 : K_ * N_ * 4;
            asm volatile("" : "+s"(so0), "+s"(stA), "+s"(stB), "+s"(nb0));
            asm volatile("" : "+v"(voff));
            PIN_ARGS();
            auto r0 = __builtin_amdgcn_make_buffer_rsrc((void*)(conv ? qx : qw), 0, nb0, 0x00020000);
            Loader L;
            unsigned xh = 0;
#pragma unroll
            for (int n = 0; n < 8; ++n) {
                L.wr[n >> 2][n & 3] = __builtin_amdgcn_raw_buffer_load_b128(r0, voff, so0 + (n & 3) * stA + (n >> 2) * stB, 0);
                FENCE();
                if (n == 7) {
                    auto rx = __builtin_amdgcn_make_buffer_rsrc((void*)qx, 0, M_ * K_ * 4, 0x00020000);
                    int l2 = lane, i2 = idx;
                    asm volatile("" : "+v"(l2));
                    asm volatile("" : "+s"(i2));
                    int Lh = (i2 - 4) * 37 + (l2 < 37 ? l2 : 36);
                    Lh = Lh < 1024 ? Lh : 1023;
                    unsigned hoff = ((Lh >> 8) * 128 + ((Lh >> 4) & 15)) * 4096 + (Lh & 15) * 128;
                    asm volatile("" : "+v"(hoff));
                    hoff = (wv == 0 && i2 >= 4) ? hoff : 0x7ffffff0u;
                    xh = __builtin_amdgcn_raw_buffer_load_b32(rx, hoff, grp * 65536 + 2048, 0);
                    FENCE();
                }
            }
            if (conv) {
                const int r8 = lane >> 3, q8 = lane & 7;
                auto rx8 = __builtin_amdgcn_make_buffer_rsrc((void*)(ws + WS_X8), 0, M_ * K_, 0x00020000);
                char* xt = ldsb + wv * 2048;
#pragma unroll
                for (int hh = 0; hh < 2; ++hh) {
                    v4u p;
#pragma unroll
                    for (int i = 0; i < 4; ++i) { const v4u v = L.wr[hh][i]; p[i] = pack4(v[0], v[1], v[2], v[3]); }
                    *(v4u*)(xt + q8 * 256 + (((8 * hh + r8) ^ q8) * 16)) = p;
                }
#pragma unroll
                for (int s = 0; s < 2; ++s) {
                    const int q = 4 * s + g;
                    const v4u a = *(const v4u*)(xt + q * 256 + ((c ^ q) * 16));
                    __builtin_amdgcn_raw_buffer_store_b128(a, rx8, ((bid * 16 + 2 * wv + s) * 64 + lane) * 16, 0, SC1);
                }
                asm volatile("" :: "v"(xh));
                VMWAIT(0);
                BAR();
                return;
            }
            auto rw = __builtin_amdgcn_make_buffer_rsrc((void*)qw, 0, K_ * N_ * 4, 0x00020000);
            const unsigned soA = kstep_off(wv);
#pragma unroll
            for (int n = 8; n < 16; ++n) {
                L.wr[n >> 2][n & 3] = __builtin_amdgcn_raw_buffer_load_b128(rw, voff, soA + (32 * (n >> 2) + (n & 3)) * (N_ * 4), 0);
                FENCE();
            }
            BAR();
            L.cs[0] = L.cs[1] = L.cs[2] = L.cs[3] = 0;
            L.packq(0);
            VMWAIT(8);
            if (lane == 0) atomicAdd(&lds[LDS_CTL / 4 + C_OWN], 1u);
            L.packq(1); L.packq(2); L.packq(3);
            if (lane == 0) atomicAdd(&lds[LDS_CTL / 4 + C_OWN], 1u);
            L.stash(lds, wv, lane);
            asm volatile("" :: "v"(xh));
            const unsigned soB = kstep_off(wv + 4);
#pragma unroll
            for (int n = 0; n < 16; ++n) {
                L.wr[4 + (n >> 2)][n & 3] = __builtin_amdgcn_raw_buffer_load_b128(rw, voff, soB + (32 * (n >> 2) + (n & 3)) * (N_ * 4), 0);
                FENCE();
            }
            L.finish(lds, wv + 4, wv, lane);
            return;
        }
        PIN_ARGS();
        if (conv) {
            if (wv > 8) {
                const u64 base = pc0 & ~4095ull;
                const unsigned off = (wv - 9) * 4096 + lane * 64;
                if (off < CODE_HINT_BYTES) { const unsigned v = *(const __attribute__((address_space(1))) unsigned*)(base + off); asm volatile("" :: "v"(v)); }
            }
            if (wv != 8) return;
            const u64 tag = ld_agent(ws) + TAG_STEP;
            BAR();
            if (lane == 0) { st_agent(ws + WS_FLAG + bid * FLAG_STRIDE, tag); st_agent(ws + WS_ACK + bid * 8, tag); }
            if (bid == 0) {
                __builtin_amdgcn_s_sleep(60);
#pragma nounroll
                for (unsigned spin = 0; spin < SPIN_CAP; ++spin) {
                    bool ok = true;
#pragma unroll
                    for (int k = 0; k < 4; ++k) ok &= (ld_agent(ws + WS_ACK + (k * 64 + lane) * 8) == tag);
                    if (__all(ok)) break;
                    __builtin_amdgcn_s_sleep(8);
                }
                if (lane == 0) st_agent(ws, tag);
            }
            return;
        }
        if (wv < 8) {
            BAR();
            auto rw = __builtin_amdgcn_make_buffer_rsrc((void*)qw, 0, K_ * N_ * 4, 0x00020000);
            const unsigned soA = kstep_off(wv + 4), soB = kstep_off(wv + 8);
            const unsigned voff = g * (4 * N_ * 4) + c * 16;
            Loader L;
            L.cs[0] = L.cs[1] = L.cs[2] = L.cs[3] = 0;
            auto ld1 = [&](int n) {
                L.wr[n >> 2][n & 3] = __builtin_amdgcn_raw_buffer_load_b128(rw, voff, (n < 16 ? soA : soB) + (32 * ((n >> 2) & 3) + (n & 3)) * (N_ * 4), 0);
                FENCE();
            };
            lds_wait_ge(ctl + C_OWN, 4);
#pragma unroll
            for (int n = 0; n < 16; ++n) ld1(n);
#pragma unroll
            for (int n = 0; n < 16; ++n) {
                VMWAIT(15);
                ld1(n + 16);
                if ((n & 3) == 3) L.packq(n >> 2);
            }
            L.stash(lds, wv + 4, lane);
            L.finish(lds, wv + 8, wv, lane);
            return;
        }
    }

    const int mw = wv - 8;
    u64 tag;
    if (mw == 0) {
        tag = ld_agent(ws);
        const unsigned par = *(const unsigned*)((lane < 32 ? swp : biasp) + n0 + 2 * (lane & 31));
        const unsigned psx = __builtin_nontemporal_load(sxp);
        const unsigned pzx = __builtin_nontemporal_load(zxp);
        FENCE();
        lds[LDS_CTL / 4 + lane] = 0;
        BAR();
        tag += TAG_STEP;
        lds[LDS_PAR / 4 + lane] = par;
        lds[LDS_PAR / 4 + 64] = psx;
        lds[LDS_PAR / 4 + 65] = pzx;
        FENCE();
        ctl[C_TAG_LO] = (unsigned)tag; ctl[C_TAG_HI] = (unsigned)(tag >> 32); ctl[C_PAR] = 1;
        if (lane == 0) st_agent(ws + WS_ACK + bid * 8, tag);
    } else {
        BAR();
        lds_wait_ge(ctl + C_PAR, 1);
        tag = ((u64)ctl[C_TAG_HI] << 32) | ctl[C_TAG_LO];
    }
    auto rx8 = __builtin_amdgcn_make_buffer_rsrc((void*)(ws + WS_X8), 0, M_ * K_, 0x00020000);
    const unsigned x8off = (mt * 8 + 2 * mw) * 16384 + lane * 16;
    v4i xa[16][2], bf[16][4], acc[2][4];
    auto ldx = [&](int u, int m) {
        xa[u][m] = __builtin_bit_cast(v4i, __builtin_amdgcn_raw_buffer_load_b128(rx8, x8off, ((4 * mt + u) & 15) * 1024 + m * 16384, SC1));
        FENCE();
    };
    auto ldb = [&](int u, int t) { bf[u][t] = *(const v4i*)(ldsb + u * 4096 + t * 1024 + lane * 16); FENCE(); };
#pragma unroll
    for (int t = 0; t < 8; ++t) acc[t >> 2][t & 3] = (v4i){0, 0, 0, 0};
    lds_wait_ge(ctl + C_OWN, 8);
    poll_flags(ws + WS_FLAG + (mt * 8 + 2 * mw + (lane & 1)) * FLAG_STRIDE, tag);
#pragma unroll
    for (int p = 0; p < 2 * XD; ++p) ldx(p >> 1, p & 1);
    v4f scv, ofv;
    unsigned rdy = 0;
    lds_wait_ge(ctl + C_GROUP, 4);
#pragma unroll
    for (int t = 0; t < 4; ++t) ldb(0, t);
#pragma unroll
    for (int u = 0; u < 16; ++u) {
#pragma unroll
        for (int k = 0; k < 8; ++k) {
            acc[k >> 2][k & 3] = __builtin_amdgcn_mfma_i32_16x16x64_i8(xa[u][k >> 2], bf[u][k & 3], acc[k >> 2][k & 3], 0, 0, 0);
            FENCE();
            if (k == 0 && u + 1 < 16 && ((u + 1) & 3) == 0) {
                if ((unsigned)__builtin_amdgcn_readfirstlane(rdy) < 4) lds_wait_ge(ctl + C_GROUP + ((u + 1) >> 2), 4);
                FENCE();
            }
            if (k < 4 && u + 1 < 16) ldb(u + 1, k);
            if ((k == 4 || k == 5) && u + XD < 16) ldx(u + XD, k - 4);
            if (k == 6 && u + 2 < 16 && ((u + 2) & 3) == 0) { rdy = ctl[C_GROUP + ((u + 2) >> 2)]; FENCE(); }
            if (k == 6 && u == 13) { rdy = ctl[C_EPI]; FENCE(); }
            if (k == 4 && u == 14) {
                if (__builtin_amdgcn_readfirstlane(rdy) == 0) lds_wait_ge(ctl + C_EPI, 1);
                FENCE();
                scv = *(const v4f*)(ldsb + LDS_EPI + 16 * c);
                ofv = *(const v4f*)(ldsb + LDS_EPI + 256 + 16 * c);
                FENCE();
            }
        }
    }
    auto ro = __builtin_amdgcn_make_buffer_rsrc((void*)out, 0, M_ * N_ * 2, 0x00020000);
    const unsigned ooff = ((mt * BM + 32 * mw + 4 * g) * N_ + n0 + 4 * c) * 2;
#pragma unroll
    for (int m = 0; m < 2; ++m)
#pragma unroll
        for (int r = 0; r < 4; ++r) {
            v2u o;
#pragma unroll
            for (int p = 0; p < 2; ++p) {
                const v2f v = (v2f){(float)acc[m][2 * p][r], (float)acc[m][2 * p + 1][r]} * (v2f){scv[2 * p], scv[2 * p + 1]}
                              + (v2f){ofv[2 * p], ofv[2 * p + 1]};
                o[p] = __builtin_bit_cast(unsigned, __builtin_convertvector(v, v2bf));
            }
            __builtin_amdgcn_raw_buffer_store_b64(o, ro, ooff, (16 * m + r) * N_ * 2, SC1);
        }
}

extern "C" void kernel_launch(void* const* d_in, const int* in_sizes, int n_in,
                              void* d_out, int out_size, void* d_ws, size_t ws_size,
                              hipStream_t stream) {
    w8a8_kernel<<<dim3(8, 4, 8), NT, 0, stream>>>((const int*)d_in[0], (const int*)d_in[1],
                                         (const unsigned short*)d_in[2], (const int*)d_in[3],
                                         (const unsigned short*)d_in[4], (const unsigned short*)d_in[5],
                                         (unsigned short*)d_out, (char*)d_ws);
}
```

```cpp
#define HP 0
#define NH 2
#define CDELAY 0
#include <hip/hip_runtime.h>
#include <hip/hip_bf16.h>
#include <stdint.h>

typedef int v4i __attribute__((ext_vector_type(4)));
typedef unsigned v4u __attribute__((ext_vector_type(4)));
typedef unsigned v2u __attribute__((ext_vector_type(2)));
typedef float v2f __attribute__((ext_vector_type(2)));
typedef float v4f __attribute__((ext_vector_type(4)));
typedef __bf16 v2bf __attribute__((ext_vector_type(2)));
typedef unsigned long long u64;
typedef __attribute__((address_space(1))) u64 gu64;
typedef __attribute__((address_space(3))) volatile unsigned lds_vu32;

constexpr int M_ = 512, K_ = 1024, N_ = 3584;
constexpr int BM = 128, BN = 64, NT = 12 * 64;
constexpr unsigned FLAG_STRIDE = 256, WS_FLAG = 4096, WS_ACK = 32768, WS_X8 = 262144;
constexpr u64 TAG_STEP = 0x9E3779B97F4A7C15ull;
constexpr int SC1 = 16;
constexpr unsigned SPIN_CAP = 1u << 15;
constexpr int LDS_CTL = 65536, LDS_PAR = LDS_CTL + 256, LDS_CS = LDS_PAR + 512, LDS_EPI = LDS_CS + 2048;
constexpr int LDS_BYTES = 84 * 1024;
enum { C_TAG_LO = 0, C_TAG_HI = 1, C_PAR = 2, C_OWN = 4, C_GROUP = 8, C_CSUM = 12, C_EPI = 13 };
constexpr unsigned CODE_HINT_BYTES = 10240;
constexpr int XD = 4;

#define PIN_ARGS() asm volatile("" :: "s"(qx), "s"(qw), "s"(sxp), "s"(zxp), "s"(swp), "s"(biasp), "s"(out), "s"(ws))
#define RT(x) asm volatile("s_memrealtime %0\n\ts_waitcnt lgkmcnt(0)" : "=s"(x) :: "memory")
#define SPINOUT() do { const u64 te_ = t0_ + 10000 + 10 * (tp_ - t0_); while (__builtin_amdgcn_s_memrealtime() < te_) __builtin_amdgcn_s_sleep(1); } while (0)
#define FENCE() do { asm volatile("" ::: "memory"); __builtin_amdgcn_sched_barrier(0); } while (0)
#define VMWAIT(n) do { asm volatile("s_waitcnt vmcnt(%0)" :: "i"(n) : "memory"); __builtin_amdgcn_sched_barrier(0); } while (0)
#define LGKM0() do { asm volatile("s_waitcnt lgkmcnt(0)" ::: "memory"); __builtin_amdgcn_sched_barrier(0); } while (0)
#define BAR() do { asm volatile("s_waitcnt lgkmcnt(0)\n\ts_barrier" ::: "memory"); __builtin_amdgcn_sched_barrier(0); } while (0)

__device__ __forceinline__ unsigned pack4(unsigned a, unsigned b, unsigned c, unsigned d) {
    return __builtin_amdgcn_perm(b, a, 0x0c0c0400u) | __builtin_amdgcn_perm(d, c, 0x04000c0cu);
}
__device__ __forceinline__ float rnd_bf16(float v) {
    const v2bf b = __builtin_convertvector((v2f){v, 0.f}, v2bf);
    return __builtin_bit_cast(float, __builtin_bit_cast(unsigned, b) << 16);
}
__device__ __forceinline__ float bf2f(unsigned short h) { return __builtin_bit_cast(float, (unsigned)h << 16); }
__device__ __forceinline__ u64 ld_agent(const void* p) { return __hip_atomic_load((gu64*)p, __ATOMIC_RELAXED, __HIP_MEMORY_SCOPE_AGENT); }
__device__ __forceinline__ void st_agent(void* p, u64 v) { __hip_atomic_store((gu64*)p, v, __ATOMIC_RELAXED, __HIP_MEMORY_SCOPE_AGENT); }
__device__ __forceinline__ void lds_wait_ge(lds_vu32* w, unsigned v) {
#pragma nounroll
    for (unsigned spin = 0; spin < (SPIN_CAP << 4); ++spin) {
        if ((unsigned)__builtin_amdgcn_readfirstlane(*w) >= v) break;
        __builtin_amdgcn_s_sleep(1);
    }
    FENCE();
}
__device__ __forceinline__ void poll_flags(const char* p, u64 tag) {
#pragma nounroll
    for (unsigned spin = 0; spin < SPIN_CAP; ++spin) {
        if (__all(ld_agent(p) == tag)) break;
        __builtin_amdgcn_s_sleep(8);
    }
    FENCE();
}

struct Loader {
    v4u wr[8][4], pk[4];
    int cs[4];
    __device__ __forceinline__ void packq(int i) {
#pragma unroll
        for (int t = 0; t < 4; ++t) {
            unsigned p = pack4(wr[i][0][t], wr[i][1][t], wr[i][2][t], wr[i][3][t]);
            cs[t] = __builtin_amdgcn_sdot4(p, 0x01010101, cs[t], false);
            asm volatile("" : "+v"(p), "+v"(cs[t]));
            pk[t][i & 3] = p;
        }
        FENCE();
    }
    __device__ __forceinline__ void stash(unsigned* lds, int slot, int lane) {
#pragma unroll
        for (int t = 0; t < 4; ++t) *(v4u*)((char*)lds + slot * 4096 + t * 1024 + lane * 16) = pk[t];
        LGKM0();
        if (lane == 0) atomicAdd(&lds[LDS_CTL / 4 + C_GROUP + (slot >> 2)], 1u);
        FENCE();
    }
    __device__ __forceinline__ void finish(unsigned* lds, int slot, int wv, int lane) {
        char* ldsb = (char*)lds;
        lds_vu32* ctl = (lds_vu32*)(lds + LDS_CTL / 4);
        packq(4); packq(5); packq(6); packq(7);
        stash(lds, slot, lane);
#pragma unroll
        for (int t = 0; t < 4; ++t) {
            auto r = __builtin_amdgcn_permlane32_swap(cs[t], cs[t], false, false);
            cs[t] = r[0] + r[1];
            r = __builtin_amdgcn_permlane16_swap(cs[t], cs[t], false, false);
            cs[t] = r[0] + r[1];
        }
        if (lane < 16) *(v4i*)(ldsb + LDS_CS + wv * 256 + lane * 16) = (v4i){cs[0], cs[1], cs[2], cs[3]};
        LGKM0();
        unsigned n = 0;
        if (lane == 0) n = atomicAdd(&lds[LDS_CTL / 4 + C_CSUM], 1u);
        if (__builtin_amdgcn_readfirstlane(n) == 7) {
            FENCE();
            int cst = 0;
#pragma unroll
            for (int k = 0; k < 8; ++k) cst += *(const int*)(ldsb + LDS_CS + k * 256 + lane * 4);
            lds_wait_ge(ctl + C_PAR, 1);
            const float sw = bf2f(*(const unsigned short*)(ldsb + LDS_PAR + 2 * lane));
            const float bi = bf2f(*(const unsigned short*)(ldsb + LDS_PAR + 128 + 2 * lane));
            const float sx = bf2f((unsigned short)lds[LDS_PAR / 4 + 64]);
            const float zc = rnd_bf16(rnd_bf16((float)cst) * rnd_bf16((float)(int)lds[LDS_PAR / 4 + 65]));
            const float sc = sw * sx;
            *(float*)(ldsb + LDS_EPI + lane * 4) = sc;
            *(float*)(ldsb + LDS_EPI + 256 + lane * 4) = bi - zc * sc;
            LGKM0();
            ctl[C_EPI] = 1;
        }
    }
};

__global__ __launch_bounds__(NT) __attribute__((aligned(4096)))
void w8a8_kernel(const int* __restrict__ qx, const int* __restrict__ qw,
                 const unsigned short* __restrict__ sxp, const int* __restrict__ zxp,
                 const unsigned short* __restrict__ swp, const unsigned short* __restrict__ biasp,
                 unsigned short* __restrict__ out, char* __restrict__ ws) {
    __shared__ __attribute__((aligned(16))) unsigned lds[LDS_BYTES / 4];
    u64 pc0;
    asm volatile("s_getpc_b64 %0" : "=s"(pc0));
    const int lane = threadIdx.x & 63;
    const int wv = __builtin_amdgcn_readfirstlane(threadIdx.x >> 6);
    const int grp = blockIdx.x, mt = blockIdx.y, bz = blockIdx.z;
    const int idx = bz * 4 + mt, bid = idx * 8 + grp;
    const int g = lane >> 4, c = lane & 15;
    char* ldsb = (char*)lds;
    lds_vu32* ctl = (lds_vu32*)(lds + LDS_CTL / 4);
    const bool conv = bz == 0;
    const int n0 = (grp * 7 + bz - 1) * BN;
    auto kstep_off = [&](int slot) { const int s = (4 * mt + slot) & 15; return (unsigned)((128 * (s >> 1) + 16 * (s & 1)) * (N_ * 4) + n0 * 4); };

    {
        if (__builtin_expect(wv < (conv ? 8 : 4), 1)) {
            unsigned soX = (mt * 8 + grp) * (16 * K_ * 4) + wv * 512;
            unsigned soW = (256 * mt + 128 * (wv >> 1) + 16 * (wv & 1)) * (N_ * 4) + n0 * 4;
            asm volatile("" : "+s"(soX), "+s"(soW));
            const unsigned sh = conv ? 3 : 4, rowb = conv ? K_ * 4 : 4 * N_ * 4;
            unsigned voff = __umul24(lane >> sh, rowb) + (lane & ((1u << sh) - 1)) * 16;
            unsigned so0 = conv ? soX : soW;
            unsigned stA = conv ? 128 : N_ * 4, stB = conv ? 8 * K_ * 4 : 32 * N_ * 4;
            unsigned nb0 = conv ? M_ * K_ * 4 : K_ * N_ * 4;
            asm volatile("" : "+s"(so0), "+s"(stA), "+s"(stB), "+s"(nb0));
            asm volatile("" : "+v"(voff));
            Loader L;
            unsigned xh = 0;
            auto hint = [&]() {
                auto rx = __builtin_amdgcn_make_buffer_rsrc((void*)qx, 0, M_ * K_ * 4, 0x00020000);
                int l2 = lane, i2 = idx;
                if (HP) { asm volatile("" : "+v"(l2)); asm volatile("" : "+s"(i2)); }
                int Lh = (i2 - 4) * 37 + (l2 < 37 ? l2 : 36);
                Lh = Lh < 1024 ? Lh : 1023;
                unsigned hoff = ((Lh >> 8) * 128 + ((Lh >> 4) & 15)) * 4096 + (Lh & 15) * 128;
                asm volatile("" : "+v"(hoff));
                hoff = (wv < NH && i2 >= 4) ? hoff : 0x7ffffff0u;
                return hoff;
            };
            unsigned hoff0 = 0;
            if (HP == 0) hoff0 = hint();
            PIN_ARGS();
            auto r0 = __builtin_amdgcn_make_buffer_rsrc((void*)(conv ? qx : qw), 0, nb0, 0x00020000);
            auto rxh = __builtin_amdgcn_make_buffer_rsrc((void*)qx, 0, M_ * K_ * 4, 0x00020000);
            if (CDELAY && conv) __builtin_amdgcn_s_sleep(CDELAY);
            if (HP == 0) { xh = __builtin_amdgcn_raw_buffer_load_b32(rxh, hoff0, grp * 65536 + (wv ? 0 : 2048), 0); FENCE(); }
#pragma unroll
            for (int n = 0; n < 8; ++n) {
                L.wr[n >> 2][n & 3] = __builtin_amdgcn_raw_buffer_load_b128(r0, voff, so0 + (n & 3) * stA + (n >> 2) * stB, 0);
                FENCE();
                if (n + 1 == HP) { xh = __builtin_amdgcn_raw_buffer_load_b32(rxh, hint(), grp * 65536 + (wv ? 0 : 2048), 0); FENCE(); }
            }
            if (conv) {
                const int r8 = lane >> 3, q8 = lane & 7;
                auto rx8 = __builtin_amdgcn_make_buffer_rsrc((void*)(ws + WS_X8), 0, M_ * K_, 0x00020000);
                char* xt = ldsb + wv * 2048;
#pragma unroll
                for (int hh = 0; hh < 2; ++hh) {
                    v4u p;
#pragma unroll
                    for (int i = 0; i < 4; ++i) { const v4u v = L.wr[hh][i]; p[i] = pack4(v[0], v[1], v[2], v[3]); }
                    *(v4u*)(xt + q8 * 256 + (((8 * hh + r8) ^ q8) * 16)) = p;
                }
#pragma unroll
                for (int s = 0; s < 2; ++s) {
                    const int q = 4 * s + g;
                    const v4u a = *(const v4u*)(xt + q * 256 + ((c ^ q) * 16));
                    __builtin_amdgcn_raw_buffer_store_b128(a, rx8, ((bid * 16 + 2 * wv + s) * 64 + lane) * 16, 0, SC1);
                }
                asm volatile("" :: "v"(xh));
                VMWAIT(0);
                BAR();
                return;
            }
            BAR();
            auto rw = __builtin_amdgcn_make_buffer_rsrc((void*)qw, 0, K_ * N_ * 4, 0x00020000);
            const unsigned soA = kstep_off(wv);
#pragma unroll
            for (int n = 8; n < 16; ++n) {
                L.wr[n >> 2][n & 3] = __builtin_amdgcn_raw_buffer_load_b128(rw, voff, soA + (32 * (n >> 2) + (n & 3)) * (N_ * 4), 0);
                FENCE();
            }
            L.cs[0] = L.cs[1] = L.cs[2] = L.cs[3] = 0;
            L.packq(0);
            VMWAIT(8);
            if (lane == 0) atomicAdd(&lds[LDS_CTL / 4 + C_OWN], 1u);
            L.packq(1); L.packq(2); L.packq(3);
            if (lane == 0) atomicAdd(&lds[LDS_CTL / 4 + C_OWN], 1u);
            L.stash(lds, wv, lane);
            asm volatile("" :: "v"(xh));
            const unsigned soB = kstep_off(wv + 4);
#pragma unroll
            for (int n = 0; n < 16; ++n) {
                L.wr[4 + (n >> 2)][n & 3] = __builtin_amdgcn_raw_buffer_load_b128(rw, voff, soB + (32 * (n >> 2) + (n & 3)) * (N_ * 4), 0);
                FENCE();
            }
            L.finish(lds, wv + 4, wv, lane);
            return;
        }
        PIN_ARGS();
        if (conv) {
            if (wv > 8) {
                const u64 base = pc0 & ~4095ull;
                const unsigned off = (wv - 9) * 4096 + lane * 64;
                if (off < CODE_HINT_BYTES) { const unsigned v = *(const __attribute__((address_space(1))) unsigned*)(base + off); asm volatile("" :: "v"(v)); }
            }
            if (wv != 8) return;
            u64 t0_ = __builtin_amdgcn_s_memrealtime(), tp_ = 0;
            const u64 tag = ld_agent(ws) + TAG_STEP;
            BAR();
            RT(tp_);
            if (lane == 0) { st_agent(ws + WS_FLAG + bid * FLAG_STRIDE, tag); st_agent(ws + WS_ACK + bid * 8, tag); }
            if (bid == 0) {
                __builtin_amdgcn_s_sleep(60);
#pragma nounroll
                for (unsigned spin = 0; spin < SPIN_CAP; ++spin) {
                    bool ok = true;
#pragma unroll
                    for (int k = 0; k < 4; ++k) ok &= (ld_agent(ws + WS_ACK + (k * 64 + lane) * 8) == tag);
                    if (__all(ok)) break;
                    __builtin_amdgcn_s_sleep(8);
                }
                if (lane == 0) st_agent(ws, tag);
            }
            SPINOUT();
            return;
        }
        if (wv < 8) {
            BAR();
            auto rw = __builtin_amdgcn_make_buffer_rsrc((void*)qw, 0, K_ * N_ * 4, 0x00020000);
            const unsigned soA = kstep_off(wv + 4), soB = kstep_off(wv + 8);
            const unsigned voff = g * (4 * N_ * 4) + c * 16;
            Loader L;
            L.cs[0] = L.cs[1] = L.cs[2] = L.cs[3] = 0;
            auto ld1 = [&](int n) {
                L.wr[n >> 2][n & 3] = __builtin_amdgcn_raw_buffer_load_b128(rw, voff, (n < 16 ? soA : soB) + (32 * ((n >> 2) & 3) + (n & 3)) * (N_ * 4), 0);
                FENCE();
            };
            lds_wait_ge(ctl + C_OWN, 4);
#pragma unroll
            for (int n = 0; n < 16; ++n) ld1(n);
#pragma unroll
            for (int n = 0; n < 16; ++n) {
                VMWAIT(15);
                ld1(n + 16);
                if ((n & 3) == 3) L.packq(n >> 2);
            }
            L.stash(lds, wv + 4, lane);
            L.finish(lds, wv + 8, wv, lane);
            return;
        }
    }

    const int mw = wv - 8;
    u64 tag;
    if (mw == 0) {
        tag = ld_agent(ws);
        const unsigned par = *(const unsigned*)((lane < 32 ? swp : biasp) + n0 + 2 * (lane & 31));
        const unsigned psx = __builtin_nontemporal_load(sxp);
        const unsigned pzx = __builtin_nontemporal_load(zxp);
        FENCE();
        lds[LDS_CTL / 4 + lane] = 0;
        BAR();
        tag += TAG_STEP;
        lds[LDS_PAR / 4 + lane] = par;
        lds[LDS_PAR / 4 + 64] = psx;
        lds[LDS_PAR / 4 + 65] = pzx;
        FENCE();
        ctl[C_TAG_LO] = (unsigned)tag; ctl[C_TAG_HI] = (unsigned)(tag >> 32); ctl[C_PAR] = 1;
        if (lane == 0) st_agent(ws + WS_ACK + bid * 8, tag);
    } else {
        BAR();
        lds_wait_ge(ctl + C_PAR, 1);
        tag = ((u64)ctl[C_TAG_HI] << 32) | ctl[C_TAG_LO];
    }
    auto rx8 = __builtin_amdgcn_make_buffer_rsrc((void*)(ws + WS_X8), 0, M_ * K_, 0x00020000);
    const unsigned x8off = (mt * 8 + 2 * mw) * 16384 + lane * 16;
    v4i xa[16][2], bf[16][4], acc[2][4];
    auto ldx = [&](int u, int m) {
        xa[u][m] = __builtin_bit_cast(v4i, __builtin_amdgcn_raw_buffer_load_b128(rx8, x8off, ((4 * mt + u) & 15) * 1024 + m * 16384, SC1));
        FENCE();
    };
    auto ldb = [&](int u, int t) { bf[u][t] = *(const v4i*)(ldsb + u * 4096 + t * 1024 + lane * 16); FENCE(); };
#pragma unroll
    for (int t = 0; t < 8; ++t) acc[t >> 2][t & 3] = (v4i){0, 0, 0, 0};
    lds_wait_ge(ctl + C_OWN, 8);
    poll_flags(ws + WS_FLAG + (mt * 8 + 2 * mw + (lane & 1)) * FLAG_STRIDE, tag);
#pragma unroll
    for (int p = 0; p < 2 * XD; ++p) ldx(p >> 1, p & 1);
    v4f scv, ofv;
    unsigned rdy = 0;
    lds_wait_ge(ctl + C_GROUP, 4);
#pragma unroll
    for (int t = 0; t < 4; ++t) ldb(0, t);
#pragma unroll
    for (int u = 0; u < 16; ++u) {
#pragma unroll
        for (int k = 0; k < 8; ++k) {
            acc[k >> 2][k & 3] = __builtin_amdgcn_mfma_i32_16x16x64_i8(xa[u][k >> 2], bf[u][k & 3], acc[k >> 2][k & 3], 0, 0, 0);
            FENCE();
            if (k == 0 && u + 1 < 16 && ((u + 1) & 3) == 0) {
                if ((unsigned)__builtin_amdgcn_readfirstlane(rdy) < 4) lds_wait_ge(ctl + C_GROUP + ((u + 1) >> 2), 4);
                FENCE();
            }
            if (k < 4 && u + 1 < 16) ldb(u + 1, k);
            if ((k == 4 || k == 5) && u + XD < 16) ldx(u + XD, k - 4);
            if (k == 6 && u + 2 < 16 && ((u + 2) & 3) == 0) { rdy = ctl[C_GROUP + ((u + 2) >> 2)]; FENCE(); }
            if (k == 6 && u == 13) { rdy = ctl[C_EPI]; FENCE(); }
            if (k == 4 && u == 14) {
                if (__builtin_amdgcn_readfirstlane(rdy) == 0) lds_wait_ge(ctl + C_EPI, 1);
                FENCE();
                scv = *(const v4f*)(ldsb + LDS_EPI + 16 * c);
                ofv = *(const v4f*)(ldsb + LDS_EPI + 256 + 16 * c);
                FENCE();
            }
        }
    }
    auto ro = __builtin_amdgcn_make_buffer_rsrc((void*)out, 0, M_ * N_ * 2, 0x00020000);
    const unsigned ooff = ((mt * BM + 32 * mw + 4 * g) * N_ + n0 + 4 * c) * 2;
#pragma unroll
    for (int m = 0; m < 2; ++m)
#pragma unroll
        for (int r = 0; r < 4; ++r) {
            v2u o;
#pragma unroll
            for (int p = 0; p < 2; ++p) {
                const v2f v = (v2f){(float)acc[m][2 * p][r], (float)acc[m][2 * p + 1][r]} * (v2f){scv[2 * p], scv[2 * p + 1]}
                              + (v2f){ofv[2 * p], ofv[2 * p + 1]};
                o[p] = __builtin_bit_cast(unsigned, __builtin_convertvector(v, v2bf));
            }
            __builtin_amdgcn_raw_buffer_store_b64(o, ro, ooff, (16 * m + r) * N_ * 2, SC1);
        }
}

extern "C" void kernel_launch(void* const* d_in, const int* in_sizes, int n_in,
                              void* d_out, int out_size, void* d_ws, size_t ws_size,
                              hipStream_t stream) {
    w8a8_kernel<<<dim3(8, 4, 8), NT, 0, stream>>>((const int*)d_in[0], (const int*)d_in[1],
                                         (const unsigned short*)d_in[2], (const int*)d_in[3],
                                         (const unsigned short*)d_in[4], (const unsigned short*)d_in[5],
                                         (unsigned short*)d_out, (char*)d_ws);
}
```

```cpp
#define HP 0
#define NH 1
#define CDELAY 0
#include <hip/hip_runtime.h>
#include <hip/hip_bf16.h>
#include <stdint.h>

typedef int v4i __attribute__((ext_vector_type(4)));
typedef unsigned v4u __attribute__((ext_vector_type(4)));
typedef unsigned v2u __attribute__((ext_vector_type(2)));
typedef float v2f __attribute__((ext_vector_type(2)));
typedef float v4f __attribute__((ext_vector_type(4)));
typedef __bf16 v2bf __attribute__((ext_vector_type(2)));
typedef unsigned long long u64;
typedef __attribute__((address_space(1))) u64 gu64;
typedef __attribute__((address_space(3))) volatile unsigned lds_vu32;

constexpr int M_ = 512, K_ = 1024, N_ = 3584;
constexpr int BM = 128, BN = 64, NT = 12 * 64;
constexpr unsigned FLAG_STRIDE = 256, WS_FLAG = 4096, WS_ACK = 32768, WS_X8 = 262144;
constexpr u64 TAG_STEP = 0x9E3779B97F4A7C15ull;
constexpr int SC1 = 16;
constexpr unsigned SPIN_CAP = 1u << 15;
constexpr int LDS_CTL = 65536, LDS_PAR = LDS_CTL + 256, LDS_CS = LDS_PAR + 512, LDS_EPI = LDS_CS + 2048;
constexpr int LDS_BYTES = 84 * 1024;
enum { C_TAG_LO = 0, C_TAG_HI = 1, C_PAR = 2, C_OWN = 4, C_GROUP = 8, C_CSUM = 12, C_EPI = 13 };
constexpr unsigned CODE_HINT_BYTES = 10240;
constexpr int XD = 4;

#define PIN_ARGS() asm volatile("" :: "s"(qx), "s"(qw), "s"(sxp), "s"(zxp), "s"(swp), "s"(biasp), "s"(out), "s"(ws))
#define FENCE() do { asm volatile("" ::: "memory"); __builtin_amdgcn_sched_barrier(0); } while (0)
#define VMWAIT(n) do { asm volatile("s_waitcnt vmcnt(%0)" :: "i"(n) : "memory"); __builtin_amdgcn_sched_barrier(0); } while (0)
#define LGKM0() do { asm volatile("s_waitcnt lgkmcnt(0)" ::: "memory"); __builtin_amdgcn_sched_barrier(0); } while (0)
#define BAR() do { asm volatile("s_waitcnt lgkmcnt(0)\n\ts_barrier" ::: "memory"); __builtin_amdgcn_sched_barrier(0); } while (0)

__device__ __forceinline__ unsigned pack4(unsigned a, unsigned b, unsigned c, unsigned d) {
    return __builtin_amdgcn_perm(b, a, 0x0c0c0400u) | __builtin_amdgcn_perm(d, c, 0x04000c0cu);
}
__device__ __forceinline__ float rnd_bf16(float v) {
    const v2bf b = __builtin_convertvector((v2f){v, 0.f}, v2bf);
    return __builtin_bit_cast(float, __builtin_bit_cast(unsigned, b) << 16);
}
__device__ __forceinline__ float bf2f(unsigned short h) { return __builtin_bit_cast(float, (unsigned)h << 16); }
__device__ __forceinline__ u64 ld_agent(const void* p) { return __hip_atomic_load((gu64*)p, __ATOMIC_RELAXED, __HIP_MEMORY_SCOPE_AGENT); }
__device__ __forceinline__ void st_agent(void* p, u64 v) { __hip_atomic_store((gu64*)p, v, __ATOMIC_RELAXED, __HIP_MEMORY_SCOPE_AGENT); }
__device__ __forceinline__ void lds_wait_ge(lds_vu32* w, unsigned v) {
#pragma nounroll
    for (unsigned spin = 0; spin < (SPIN_CAP << 4); ++spin) {
        if ((unsigned)__builtin_amdgcn_readfirstlane(*w) >= v) break;
        __builtin_amdgcn_s_sleep(1);
    }
    FENCE();
}
__device__ __forceinline__ void poll_flags(const char* p, u64 tag) {
#pragma nounroll
    for (unsigned spin = 0; spin < SPIN_CAP; ++spin) {
        if (__all(ld_agent(p) == tag)) break;
        __builtin_amdgcn_s_sleep(8);
    }
    FENCE();
}

struct Loader {
    v4u wr[8][4], pk[4];
    int cs[4];
    __device__ __forceinline__ void packq(int i) {
#pragma unroll
        for (int t = 0; t < 4; ++t) {
            unsigned p = pack4(wr[i][0][t], wr[i][1][t], wr[i][2][t], wr[i][3][t]);
            cs[t] = __builtin_amdgcn_sdot4(p, 0x01010101, cs[t], false);
            asm volatile("" : "+v"(p), "+v"(cs[t]));
            pk[t][i & 3] = p;
        }
        FENCE();
    }
    __device__ __forceinline__ void stash(unsigned* lds, int slot, int lane) {
#pragma unroll
        for (int t = 0; t < 4; ++t) *(v4u*)((char*)lds + slot * 4096 + t * 1024 + lane * 16) = pk[t];
        LGKM0();
        if (lane == 0) atomicAdd(&lds[LDS_CTL / 4 + C_GROUP + (slot >> 2)], 1u);
        FENCE();
    }
    __device__ __forceinline__ void finish(unsigned* lds, int slot, int wv, int lane) {
        char* ldsb = (char*)lds;
        lds_vu32* ctl = (lds_vu32*)(lds + LDS_CTL / 4);
        packq(4); packq(5); packq(6); packq(7);
        stash(lds, slot, lane);
#pragma unroll
        for (int t = 0; t < 4; ++t) {
            auto r = __builtin_amdgcn_permlane32_swap(cs[t], cs[t], false, false);
            cs[t] = r[0] + r[1];
            r = __builtin_amdgcn_permlane16_swap(cs[t], cs[t], false, false);
            cs[t] = r[0] + r[1];
        }
        if (lane < 16) *(v4i*)(ldsb + LDS_CS + wv * 256 + lane * 16) = (v4i){cs[0], cs[1], cs[2], cs[3]};
        LGKM0();
        unsigned n = 0;
        if (lane == 0) n = atomicAdd(&lds[LDS_CTL / 4 + C_CSUM], 1u);
        if (__builtin_amdgcn_readfirstlane(n) == 7) {
            FENCE();
            int cst = 0;
#pragma unroll
            for (int k = 0; k < 8; ++k) cst += *(const int*)(ldsb + LDS_CS + k * 256 + lane * 4);
            lds_wait_ge(ctl + C_PAR, 1);
            const float sw = bf2f(*(const unsigned short*)(ldsb + LDS_PAR + 2 * lane));
            const float bi = bf2f(*(const unsigned short*)(ldsb + LDS_PAR + 128 + 2 * lane));
            const float sx = bf2f((unsigned short)lds[LDS_PAR / 4 + 64]);
            const float zc = rnd_bf16(rnd_bf16((float)cst) * rnd_bf16((float)(int)lds[LDS_PAR / 4 + 65]));
            const float sc = sw * sx;
            *(float*)(ldsb + LDS_EPI + lane * 4) = sc;
            *(float*)(ldsb + LDS_EPI + 256 + lane * 4) = bi - zc * sc;
            LGKM0();
            ctl[C_EPI] = 1;
        }
    }
};

__global__ __launch_bounds__(NT) __attribute__((aligned(4096)))
void w8a8_kernel(const int* __restrict__ qx, const int* __restrict__ qw,
                 const unsigned short* __restrict__ sxp, const int* __restrict__ zxp,
                 const unsigned short* __restrict__ swp, const unsigned short* __restrict__ biasp,
                 unsigned short* __restrict__ out, char* __restrict__ ws) {
    __shared__ __attribute__((aligned(16))) unsigned lds[LDS_BYTES / 4];
    u64 pc0;
    asm volatile("s_getpc_b64 %0" : "=s"(pc0));
    const int lane = threadIdx.x & 63;
    const int wv = __builtin_amdgcn_readfirstlane(threadIdx.x >> 6);
    const int grp = blockIdx.x, mt = blockIdx.y, bz = blockIdx.z;
    const int idx = bz * 4 + mt, bid = idx * 8 + grp;
    const int g = lane >> 4, c = lane & 15;
    char* ldsb = (char*)lds;
    lds_vu32* ctl = (lds_vu32*)(lds + LDS_CTL / 4);
    const bool conv = bz == 0;
    const int n0 = (grp * 7 + bz - 1) * BN;
    auto kstep_off = [&](int slot) { const int s = (4 * mt + slot) & 15; return (unsigned)((128 * (s >> 1) + 16 * (s & 1)) * (N_ * 4) + n0 * 4); };

    {
        if (__builtin_expect(wv < (conv ? 8 : 4), 1)) {
            unsigned soX = (mt * 8 + grp) * (16 * K_ * 4) + wv * 512;
            unsigned soW = (256 * mt + 128 * (wv >> 1) + 16 * (wv & 1)) * (N_ * 4) + n0 * 4;
            asm volatile("" : "+s"(soX), "+s"(soW));
            const unsigned sh = conv ? 3 : 4, rowb = conv ? K_ * 4 : 4 * N_ * 4;
            unsigned voff = __umul24(lane >> sh, rowb) + (lane & ((1u << sh) - 1)) * 16;
            unsigned so0 = conv ? soX : soW;
            unsigned stA = conv ? 128 : N_ * 4, stB = conv ? 8 * K_ * 4 : 32 * N_ * 4;
            unsigned nb0 = conv ? M_ * K_ * 4 : K_ * N_ * 4;
            asm volatile("" : "+s"(so0), "+s"(stA), "+s"(stB), "+s"(nb0));
            asm volatile("" : "+v"(voff));
            Loader L;
            unsigned xh = 0;
            auto hint = [&]() {
                auto rx = __builtin_amdgcn_make_buffer_rsrc((void*)qx, 0, M_ * K_ * 4, 0x00020000);
                int l2 = lane, i2 = idx;
                if (HP) { asm volatile("" : "+v"(l2)); asm volatile("" : "+s"(i2)); }
                int Lh = (i2 - 4) * 37 + (l2 < 37 ? l2 : 36);
                Lh = Lh < 1024 ? Lh : 1023;
                unsigned hoff = ((Lh >> 8) * 128 + ((Lh >> 4) & 15)) * 4096 + (Lh & 15) * 128;
                asm volatile("" : "+v"(hoff));
                hoff = (wv < NH && i2 >= 4) ? hoff : 0x7ffffff0u;
                return hoff;
            };
            unsigned hoff0 = 0;
            if (HP == 0) hoff0 = hint();
            PIN_ARGS();
            auto r0 = __builtin_amdgcn_make_buffer_rsrc((void*)(conv ? qx : qw), 0, nb0, 0x00020000);
            auto rxh = __builtin_amdgcn_make_buffer_rsrc((void*)qx, 0, M_ * K_ * 4, 0x00020000);
            if (CDELAY && conv) __builtin_amdgcn_s_sleep(CDELAY);
            if (HP == 0) { xh = __builtin_amdgcn_raw_buffer_load_b32(rxh, hoff0, grp * 65536 + (wv ? 0 : 2048), 0); FENCE(); }
#pragma unroll
            for (int n = 0; n < 8; ++n) {
                L.wr[n >> 2][n & 3] = __builtin_amdgcn_raw_buffer_load_b128(r0, voff, so0 + (n & 3) * stA + (n >> 2) * stB, 0);
                FENCE();
                if (n + 1 == HP) { xh = __builtin_amdgcn_raw_buffer_load_b32(rxh, hint(), grp * 65536 + (wv ? 0 : 2048), 0); FENCE(); }
            }
            if (conv) {
                const int r8 = lane >> 3, q8 = lane & 7;
                auto rx8 = __builtin_amdgcn_make_buffer_rsrc((void*)(ws + WS_X8), 0, M_ * K_, 0x00020000);
                char* xt = ldsb + wv * 2048;
#pragma unroll
                for (int hh = 0; hh < 2; ++hh) {
                    v4u p;
#pragma unroll
                    for (int i = 0; i < 4; ++i) { const v4u v = L.wr[hh][i]; p[i] = pack4(v[0], v[1], v[2], v[3]); }
                    *(v4u*)(xt + q8 * 256 + (((8 * hh + r8) ^ q8) * 16)) = p;
                }
#pragma unroll
                for (int s = 0; s < 2; ++s) {
                    const int q = 4 * s + g;
                    const v4u a = *(const v4u*)(xt + q * 256 + ((c ^ q) * 16));
                    __builtin_amdgcn_raw_buffer_store_b128(a, rx8, ((bid * 16 + 2 * wv + s) * 64 + lane) * 16, 0, SC1);
                }
                asm volatile("" :: "v"(xh));
                VMWAIT(0);
                BAR();
                return;
            }
            BAR();
            auto rw = __builtin_amdgcn_make_buffer_rsrc((void*)qw, 0, K_ * N_ * 4, 0x00020000);
            const unsigned soA = kstep_off(wv);
#pragma unroll
            for (int n = 8; n < 16; ++n) {
                L.wr[n >> 2][n & 3] = __builtin_amdgcn_raw_buffer_load_b128(rw, voff, soA + (32 * (n >> 2) + (n & 3)) * (N_ * 4), 0);
                FENCE();
            }
            L.cs[0] = L.cs[1] = L.cs[2] = L.cs[3] = 0;
            L.packq(0);
            VMWAIT(8);
            if (lane == 0) atomicAdd(&lds[LDS_CTL / 4 + C_OWN], 1u);
            L.packq(1); L.packq(2); L.packq(3);
            if (lane == 0) atomicAdd(&lds[LDS_CTL / 4 + C_OWN], 1u);
            L.stash(lds, wv, lane);
            asm volatile("" :: "v"(xh));
            const unsigned soB = kstep_off(wv + 4);
#pragma unroll
            for (int n = 0; n < 16; ++n) {
                L.wr[4 + (n >> 2)][n & 3] = __builtin_amdgcn_raw_buffer_load_b128(rw, voff, soB + (32 * (n >> 2) + (n & 3)) * (N_ * 4), 0);
                FENCE();
            }
            L.finish(lds, wv + 4, wv, lane);
            return;
        }
        PIN_ARGS();
        if (conv) {
            if (wv > 8) {
                const u64 base = pc0 & ~4095ull;
                const unsigned off = (wv - 9) * 4096 + lane * 64;
                if (off < CODE_HINT_BYTES) { const unsigned v = *(const __attribute__((address_space(1))) unsigned*)(base + off); asm volatile("" :: "v"(v)); }
            }
            if (wv != 8) return;
            const u64 tag = ld_agent(ws) + TAG_STEP;
            BAR();
            if (lane == 0) { st_agent(ws + WS_FLAG + bid * FLAG_STRIDE, tag); st_agent(ws + WS_ACK + bid * 8, tag); }
            if (bid == 0) {
                __builtin_amdgcn_s_sleep(60);
#pragma nounroll
                for (unsigned spin = 0; spin < SPIN_CAP; ++spin) {
                    bool ok = true;
#pragma unroll
                    for (int k = 0; k < 4; ++k) ok &= (ld_agent(ws + WS_ACK + (k * 64 + lane) * 8) == tag);
                    if (__all(ok)) break;
                    __builtin_amdgcn_s_sleep(8);
                }
                if (lane == 0) st_agent(ws, tag);
            }
            return;
        }
        if (wv < 8) {
            BAR();
            auto rw = __builtin_amdgcn_make_buffer_rsrc((void*)qw, 0, K_ * N_ * 4, 0x00020000);
            const unsigned soA = kstep_off(wv + 4), soB = kstep_off(wv + 8);
            const unsigned voff = g * (4 * N_ * 4) + c * 16;
            Loader L;
            L.cs[0] = L.cs[1] = L.cs[2] = L.cs[3] = 0;
            auto ld1 = [&](int n) {
                L.wr[n >> 2][n & 3] = __builtin_amdgcn_raw_buffer_load_b128(rw, voff, (n < 16 ? soA : soB) + (32 * ((n >> 2) & 3) + (n & 3)) * (N_ * 4), 0);
                FENCE();
            };
            lds_wait_ge(ctl + C_OWN, 4);
#pragma unroll
            for (int n = 0; n < 16; ++n) ld1(n);
#pragma unroll
            for (int n = 0; n < 16; ++n) {
                VMWAIT(15);
                ld1(n + 16);
                if ((n & 3) == 3) L.packq(n >> 2);
            }
            L.stash(lds, wv + 4, lane);
            L.finish(lds, wv + 8, wv, lane);
            return;
        }
    }

    const int mw = wv - 8;
    u64 tag;
    if (mw == 0) {
        tag = ld_agent(ws);
        const unsigned par = *(const unsigned*)((lane < 32 ? swp : biasp) + n0 + 2 * (lane & 31));
        const unsigned psx = __builtin_nontemporal_load(sxp);
        const unsigned pzx = __builtin_nontemporal_load(zxp);
        FENCE();
        lds[LDS_CTL / 4 + lane] = 0;
        BAR();
        tag += TAG_STEP;
        lds[LDS_PAR / 4 + lane] = par;
        lds[LDS_PAR / 4 + 64] = psx;
        lds[LDS_PAR / 4 + 65] = pzx;
        FENCE();
        ctl[C_TAG_LO] = (unsigned)tag; ctl[C_TAG_HI] = (unsigned)(tag >> 32); ctl[C_PAR] = 1;
        if (lane == 0) st_agent(ws + WS_ACK + bid * 8, tag);
    } else {
        BAR();
        lds_wait_ge(ctl + C_PAR, 1);
        tag = ((u64)ctl[C_TAG_HI] << 32) | ctl[C_TAG_LO];
    }
    auto rx8 = __builtin_amdgcn_make_buffer_rsrc((void*)(ws + WS_X8), 0, M_ * K_, 0x00020000);
    const unsigned x8off = (mt * 8 + 2 * mw) * 16384 + lane * 16;
    v4i xa[16][2], bf[16][4], acc[2][4];
    auto ldx = [&](int u, int m) {
        xa[u][m] = __builtin_bit_cast(v4i, __builtin_amdgcn_raw_buffer_load_b128(rx8, x8off, ((4 * mt + u) & 15) * 1024 + m * 16384, SC1));
        FENCE();
    };
    auto ldb = [&](int u, int t) { bf[u][t] = *(const v4i*)(ldsb + u * 4096 + t * 1024 + lane * 16); FENCE(); };
#pragma unroll
    for (int t = 0; t < 8; ++t) acc[t >> 2][t & 3] = (v4i){0, 0, 0, 0};
    lds_wait_ge(ctl + C_OWN, 8);
    poll_flags(ws + WS_FLAG + (mt * 8 + 2 * mw + (lane & 1)) * FLAG_STRIDE, tag);
#pragma unroll
    for (int p = 0; p < 2 * XD; ++p) ldx(p >> 1, p & 1);
    v4f scv, ofv;
    unsigned rdy = 0;
    lds_wait_ge(ctl + C_GROUP, 4);
#pragma unroll
    for (int t = 0; t < 4; ++t) ldb(0, t);
#pragma unroll
    for (int u = 0; u < 16; ++u) {
#pragma unroll
        for (int k = 0; k < 8; ++k) {
            acc[k >> 2][k & 3] = __builtin_amdgcn_mfma_i32_16x16x64_i8(xa[u][k >> 2], bf[u][k & 3], acc[k >> 2][k & 3], 0, 0, 0);
            FENCE();
            if (k == 0 && u + 1 < 16 && ((u + 1) & 3) == 0) {
                if ((unsigned)__builtin_amdgcn_readfirstlane(rdy) < 4) lds_wait_ge(ctl + C_GROUP + ((u + 1) >> 2), 4);
                FENCE();
            }
            if (k < 4 && u + 1 < 16) ldb(u + 1, k);
            if ((k == 4 || k == 5) && u + XD < 16) ldx(u + XD, k - 4);
            if (k == 6 && u + 2 < 16 && ((u + 2) & 3) == 0) { rdy = ctl[C_GROUP + ((u + 2) >> 2)]; FENCE(); }
            if (k == 6 && u == 13) { rdy = ctl[C_EPI]; FENCE(); }
            if (k == 4 && u == 14) {
                if (__builtin_amdgcn_readfirstlane(rdy) == 0) lds_wait_ge(ctl + C_EPI, 1);
                FENCE();
                scv = *(const v4f*)(ldsb + LDS_EPI + 16 * c);
                ofv = *(const v4f*)(ldsb + LDS_EPI + 256 + 16 * c);
                FENCE();
            }
        }
    }
    auto ro = __builtin_amdgcn_make_buffer_rsrc((void*)out, 0, M_ * N_ * 2, 0x00020000);
    const unsigned ooff = ((mt * BM + 32 * mw + 4 * g) * N_ + n0 + 4 * c) * 2;
#pragma unroll
    for (int m = 0; m < 2; ++m)
#pragma unroll
        for (int r = 0; r < 4; ++r) {
            v2u o;
#pragma unroll
            for (int p = 0; p < 2; ++p) {
                const v2f v = (v2f){(float)acc[m][2 * p][r], (float)acc[m][2 * p + 1][r]} * (v2f){scv[2 * p], scv[2 * p + 1]}
                              + (v2f){ofv[2 * p], ofv[2 * p + 1]};
                o[p] = __builtin_bit_cast(unsigned, __builtin_convertvector(v, v2bf));
            }
            __builtin_amdgcn_raw_buffer_store_b64(o, ro, ooff, (16 * m + r) * N_ * 2, SC1);
        }
}

extern "C" void kernel_launch(void* const* d_in, const int* in_sizes, int n_in,
                              void* d_out, int out_size, void* d_ws, size_t ws_size,
                              hipStream_t stream) {
    w8a8_kernel<<<dim3(8, 4, 8), NT, 0, stream>>>((const int*)d_in[0], (const int*)d_in[1],
                                         (const unsigned short*)d_in[2], (const int*)d_in[3],
                                         (const unsigned short*)d_in[4], (const unsigned short*)d_in[5],
                                         (unsigned short*)d_out, (char*)d_ws);
}
```

```cpp
#include <hip/hip_runtime.h>
#include <hip/hip_bf16.h>
#include <stdint.h>

typedef int v4i __attribute__((ext_vector_type(4)));
typedef unsigned v4u __attribute__((ext_vector_type(4)));
typedef unsigned v2u __attribute__((ext_vector_type(2)));
typedef float v2f __attribute__((ext_vector_type(2)));
typedef float v4f __attribute__((ext_vector_type(4)));
typedef __bf16 v2bf __attribute__((ext_vector_type(2)));
typedef unsigned long long u64;
typedef __attribute__((address_space(1))) u64 gu64;
typedef __attribute__((address_space(3))) volatile unsigned lds_vu32;

constexpr int M_ = 512, K_ = 1024, N_ = 3584;
constexpr int BM = 128, BN = 64, NT = 12 * 64;
constexpr unsigned FLAG_STRIDE = 256, WS_FLAG = 4096, WS_ACK = 32768, WS_X8 = 262144;
constexpr u64 TAG_STEP = 0x9E3779B97F4A7C15ull;
constexpr int SC1 = 16;
constexpr unsigned SPIN_CAP = 1u << 15;
constexpr int LDS_CTL = 65536, LDS_PAR = LDS_CTL + 256, LDS_CS = LDS_PAR + 512, LDS_EPI = LDS_CS + 2048;
constexpr int LDS_BYTES = 84 * 1024;
enum { C_TAG_LO = 0, C_TAG_HI = 1, C_PAR = 2, C_OWN = 4, C_GROUP = 8, C_CSUM = 12, C_EPI = 13 };
constexpr unsigned CODE_HINT_BYTES = 10240;
constexpr int XD = 4;

#define PIN_ARGS() asm volatile("" :: "s"(qx), "s"(qw), "s"(sxp), "s"(zxp), "s"(swp), "s"(biasp), "s"(out), "s"(ws))
#define FENCE() do { asm volatile("" ::: "memory"); __builtin_amdgcn_sched_barrier(0); } while (0)
#define VMWAIT(n) do { asm volatile("s_waitcnt vmcnt(%0)" :: "i"(n) : "memory"); __builtin_amdgcn_sched_barrier(0); } while (0)
#define LGKM0() do { asm volatile("s_waitcnt lgkmcnt(0)" ::: "memory"); __builtin_amdgcn_sched_barrier(0); } while (0)
#define BAR() do { asm volatile("s_waitcnt lgkmcnt(0)\n\ts_barrier" ::: "memory"); __builtin_amdgcn_sched_barrier(0); } while (0)

__device__ __forceinline__ unsigned pack4(unsigned a, unsigned b, unsigned c, unsigned d) {
    return __builtin_amdgcn_perm(b, a, 0x0c0c0400u) | __builtin_amdgcn_perm(d, c, 0x04000c0cu);
}
__device__ __forceinline__ float rnd_bf16(float v) {
    const v2bf b = __builtin_convertvector((v2f){v, 0.f}, v2bf);
    return __builtin_bit_cast(float, __builtin_bit_cast(unsigned, b) << 16);
}
__device__ __forceinline__ float bf2f(unsigned short h) { return __builtin_bit_cast(float, (unsigned)h << 16); }
__device__ __forceinline__ u64 ld_agent(const void* p) { return __hip_atomic_load((gu64*)p, __ATOMIC_RELAXED, __HIP_MEMORY_SCOPE_AGENT); }
__device__ __forceinline__ void st_agent(void* p, u64 v) { __hip_atomic_store((gu64*)p, v, __ATOMIC_RELAXED, __HIP_MEMORY_SCOPE_AGENT); }
__device__ __forceinline__ void lds_wait_ge(lds_vu32* w, unsigned v) {
#pragma nounroll
    for (unsigned spin = 0; spin < (SPIN_CAP << 4); ++spin) {
        if ((unsigned)__builtin_amdgcn_readfirstlane(*w) >= v) break;
        __builtin_amdgcn_s_sleep(1);
    }
    FENCE();
}
__device__ __forceinline__ void poll_flags(const char* p, u64 tag) {
#pragma nounroll
    for (unsigned spin = 0; spin < SPIN_CAP; ++spin) {
        if (__all(ld_agent(p) == tag)) break;
        __builtin_amdgcn_s_sleep(8);
    }
    FENCE();
}

struct Loader {
    v4u wr[8][4], pk[4];
    int cs[4];
    __device__ __forceinline__ void packq(int i) {
#pragma unroll
        for (int t = 0; t < 4; ++t) {
            unsigned p = pack4(wr[i][0][t], wr[i][1][t], wr[i][2][t], wr[i][3][t]);
            cs[t] = __builtin_amdgcn_sdot4(p, 0x01010101, cs[t], false);
            asm volatile("" : "+v"(p), "+v"(cs[t]));
            pk[t][i & 3] = p;
        }
        FENCE();
    }
    __device__ __forceinline__ void stash(unsigned* lds, int slot, int lane) {
#pragma unroll
        for (int t = 0; t < 4; ++t) *(v4u*)((char*)lds + slot * 4096 + t * 1024 + lane * 16) = pk[t];
        LGKM0();
        if (lane == 0) atomicAdd(&lds[LDS_CTL / 4 + C_GROUP + (slot >> 2)], 1u);
        FENCE();
    }
    __device__ __forceinline__ void finish(unsigned* lds, int slot, int wv, int lane) {
        char* ldsb = (char*)lds;
        lds_vu32* ctl = (lds_vu32*)(lds + LDS_CTL / 4);
        packq(4); packq(5); packq(6); packq(7);
        stash(lds, slot, lane);
#pragma unroll
        for (int t = 0; t < 4; ++t) {
            auto r = __builtin_amdgcn_permlane32_swap(cs[t], cs[t], false, false);
            cs[t] = r[0] + r[1];
            r = __builtin_amdgcn_permlane16_swap(cs[t], cs[t], false, false);
            cs[t] = r[0] + r[1];
        }
        if (lane < 16) *(v4i*)(ldsb + LDS_CS + wv * 256 + lane * 16) = (v4i){cs[0], cs[1], cs[2], cs[3]};
        LGKM0();
        unsigned n = 0;
        if (lane == 0) n = atomicAdd(&lds[LDS_CTL / 4 + C_CSUM], 1u);
        if (__builtin_amdgcn_readfirstlane(n) == 7) {
            FENCE();
            int cst = 0;
#pragma unroll
            for (int k = 0; k < 8; ++k) cst += *(const int*)(ldsb + LDS_CS + k * 256 + lane * 4);
            lds_wait_ge(ctl + C_PAR, 1);
            const float sw = bf2f(*(const unsigned short*)(ldsb + LDS_PAR + 2 * lane));
            const float bi = bf2f(*(const unsigned short*)(ldsb + LDS_PAR + 128 + 2 * lane));
            const float sx = bf2f((unsigned short)lds[LDS_PAR / 4 + 64]);
            const float zc = rnd_bf16(rnd_bf16((float)cst) * rnd_bf16((float)(int)lds[LDS_PAR / 4 + 65]));
            const float sc = sw * sx;
            *(float*)(ldsb + LDS_EPI + lane * 4) = sc;
            *(float*)(ldsb + LDS_EPI + 256 + lane * 4) = bi - zc * sc;
            LGKM0();
            ctl[C_EPI] = 1;
        }
    }
};

__global__ __launch_bounds__(NT) __attribute__((aligned(4096)))
void w8a8_kernel(const int* __restrict__ qx, const int* __restrict__ qw,
                 const unsigned short* __restrict__ sxp, const int* __restrict__ zxp,
                 const unsigned short* __restrict__ swp, const unsigned short* __restrict__ biasp,
                 unsigned short* __restrict__ out, char* __restrict__ ws) {
    __shared__ __attribute__((aligned(16))) unsigned lds[LDS_BYTES / 4];
    u64 pc0;
    asm volatile("s_getpc_b64 %0" : "=s"(pc0));
    const int lane = threadIdx.x & 63;
    const int wv = __builtin_amdgcn_readfirstlane(threadIdx.x >> 6);
    const int grp = blockIdx.x, mt = blockIdx.y, bz = blockIdx.z;
    const int idx = bz * 4 + mt, bid = idx * 8 + grp;
    const int g = lane >> 4, c = lane & 15;
    char* ldsb = (char*)lds;
    lds_vu32* ctl = (lds_vu32*)(lds + LDS_CTL / 4);
    const bool conv = bz == 0;
    const int n0 = (grp * 7 + bz - 1) * BN;
    auto kstep_off = [&](int slot) { const int s = (4 * mt + slot) & 15; return (unsigned)((128 * (s >> 1) + 16 * (s & 1)) * (N_ * 4) + n0 * 4); };

    {
        if (__builtin_expect(wv < (conv ? 8 : 4), 1)) {
            unsigned soX = (mt * 8 + grp) * (16 * K_ * 4) + wv * 512;
            unsigned soW = (256 * mt + 128 * (wv >> 1) + 16 * (wv & 1)) * (N_ * 4) + n0 * 4;
            asm volatile("" : "+s"(soX), "+s"(soW));
            const unsigned sh = conv ? 3 : 4, rowb = conv ? K_ * 4 : 4 * N_ * 4;
            unsigned voff = __umul24(lane >> sh, rowb) + (lane & ((1u << sh) - 1)) * 16;
            unsigned so0 = conv ? soX : soW;
            unsigned stA = conv ? 128 : N_ * 4, stB = conv ? 8 * K_ * 4 : 32 * N_ * 4;
            unsigned nb0 = conv ? M_ * K_ * 4 : K_ * N_ * 4;
            asm volatile("" : "+s"(so0), "+s"(stA), "+s"(stB), "+s"(nb0));
            asm volatile("" : "+v"(voff));
            PIN_ARGS();
            auto r0 = __builtin_amdgcn_make_buffer_rsrc((void*)(conv ? qx : qw), 0, nb0, 0x00020000);
            Loader L;
            unsigned xh = 0;
#pragma unroll
            for (int n = 0; n < 8; ++n) {
                L.wr[n >> 2][n & 3] = __builtin_amdgcn_raw_buffer_load_b128(r0, voff, so0 + (n & 3) * stA + (n >> 2) * stB, 0);
                FENCE();
                if (n == 7) {
                    auto rx = __builtin_amdgcn_make_buffer_rsrc((void*)qx, 0, M_ * K_ * 4, 0x00020000);
                    int l2 = lane, i2 = idx;
                    asm volatile("" : "+v"(l2));
                    asm volatile("" : "+s"(i2));
                    int Lh = (i2 - 4) * 37 + (l2 < 37 ? l2 : 36);
                    Lh = Lh < 1024 ? Lh : 1023;
                    unsigned hoff = ((Lh >> 8) * 128 + ((Lh >> 4) & 15)) * 4096 + (Lh & 15) * 128;
                    asm volatile("" : "+v"(hoff));
                    hoff = (wv == 0 && i2 >= 4) ? hoff : 0x7ffffff0u;
                    xh = __builtin_amdgcn_raw_buffer_load_b32(rx, hoff, grp * 65536 + 2048, 0);
                    FENCE();
                }
            }
            if (conv) {
                const int r8 = lane >> 3, q8 = lane & 7;
                auto rx8 = __builtin_amdgcn_make_buffer_rsrc((void*)(ws + WS_X8), 0, M_ * K_, 0x00020000);
                char* xt = ldsb + wv * 2048;
#pragma unroll
                for (int hh = 0; hh < 2; ++hh) {
                    v4u p;
#pragma unroll
                    for (int i = 0; i < 4; ++i) { const v4u v = L.wr[hh][i]; p[i] = pack4(v[0], v[1], v[2], v[3]); }
                    *(v4u*)(xt + q8 * 256 + (((8 * hh + r8) ^ q8) * 16)) = p;
                }
#pragma unroll
                for (int s = 0; s < 2; ++s) {
                    const int q = 4 * s + g;
                    const v4u a = *(const v4u*)(xt + q * 256 + ((c ^ q) * 16));
                    __builtin_amdgcn_raw_buffer_store_b128(a, rx8, ((bid * 16 + 2 * wv + s) * 64 + lane) * 16, 0, SC1);
                }
                asm volatile("" :: "v"(xh));
                VMWAIT(0);
                BAR();
                return;
            }
            BAR();
            auto rw = __builtin_amdgcn_make_buffer_rsrc((void*)qw, 0, K_ * N_ * 4, 0x00020000);
            const unsigned soA = kstep_off(wv);
#pragma unroll
            for (int n = 8; n < 16; ++n) {
                L.wr[n >> 2][n & 3] = __builtin_amdgcn_raw_buffer_load_b128(rw, voff, soA + (32 * (n >> 2) + (n & 3)) * (N_ * 4), 0);
                FENCE();
            }
            L.cs[0] = L.cs[1] = L.cs[2] = L.cs[3] = 0;
            L.packq(0);
            VMWAIT(8);
            if (lane == 0) atomicAdd(&lds[LDS_CTL / 4 + C_OWN], 1u);
            L.packq(1); L.packq(2); L.packq(3);
            if (lane == 0) atomicAdd(&lds[LDS_CTL / 4 + C_OWN], 1u);
            L.stash(lds, wv, lane);
            asm volatile("" :: "v"(xh));
            const unsigned soB = kstep_off(wv + 4);
#pragma unroll
            for (int n = 0; n < 16; ++n) {
                L.wr[4 + (n >> 2)][n & 3] = __builtin_amdgcn_raw_buffer_load_b128(rw, voff, soB + (32 * (n >> 2) + (n & 3)) * (N_ * 4), 0);
                FENCE();
            }
            L.finish(lds, wv + 4, wv, lane);
            return;
        }
        PIN_ARGS();
        if (conv) {
            if (wv > 8) {
                const u64 base = pc0 & ~4095ull;
                const unsigned off = (wv - 9) * 4096 + lane * 64;
                if (off < CODE_HINT_BYTES) { const unsigned v = *(const __attribute__((address_space(1))) unsigned*)(base + off); asm volatile("" :: "v"(v)); }
            }
            if (wv != 8) return;
            const u64 tag = ld_agent(ws) + TAG_STEP;
            BAR();
            if (lane == 0) { st_agent(ws + WS_FLAG + bid * FLAG_STRIDE, tag); st_agent(ws + WS_ACK + bid * 8, tag); }
            if (bid == 0) {
                __builtin_amdgcn_s_sleep(60);
#pragma nounroll
                for (unsigned spin = 0; spin < SPIN_CAP; ++spin) {
                    bool ok = true;
#pragma unroll
                    for (int k = 0; k < 4; ++k) ok &= (ld_agent(ws + WS_ACK + (k * 64 + lane) * 8) == tag);
                    if (__all(ok)) break;
                    __builtin_amdgcn_s_sleep(8);
                }
                if (lane == 0) st_agent(ws, tag);
            }
            return;
        }
        if (wv < 8) {
            BAR();
            auto rw = __builtin_amdgcn_make_buffer_rsrc((void*)qw, 0, K_ * N_ * 4, 0x00020000);
            const unsigned soA = kstep_off(wv + 4), soB = kstep_off(wv + 8);
            const unsigned voff = g * (4 * N_ * 4) + c * 16;
            Loader L;
            L.cs[0] = L.cs[1] = L.cs[2] = L.cs[3] = 0;
            auto ld1 = [&](int n) {
                L.wr[n >> 2][n & 3] = __builtin_amdgcn_raw_buffer_load_b128(rw, voff, (n < 16 ? soA : soB) + (32 * ((n >> 2) & 3) + (n & 3)) * (N_ * 4), 0);
                FENCE();
            };
            lds_wait_ge(ctl + C_OWN, 4);
#pragma unroll
            for (int n = 0; n < 16; ++n) ld1(n);
#pragma unroll
            for (int n = 0; n < 16; ++n) {
                VMWAIT(15);
                ld1(n + 16);
                if ((n & 3) == 3) L.packq(n >> 2);
            }
            L.stash(lds, wv + 4, lane);
            L.finish(lds, wv + 8, wv, lane);
            return;
        }
    }

    const int mw = wv - 8;
    u64 tag;
    if (mw == 0) {
        tag = ld_agent(ws);
        const unsigned par = *(const unsigned*)((lane < 32 ? swp : biasp) + n0 + 2 * (lane & 31));
        const unsigned psx = __builtin_nontemporal_load(sxp);
        const unsigned pzx = __builtin_nontemporal_load(zxp);
        FENCE();
        lds[LDS_CTL / 4 + lane] = 0;
        BAR();
        tag += TAG_STEP;
        lds[LDS_PAR / 4 + lane] = par;
        lds[LDS_PAR / 4 + 64] = psx;
        lds[LDS_PAR / 4 + 65] = pzx;
        FENCE();
        ctl[C_TAG_LO] = (unsigned)tag; ctl[C_TAG_HI] = (unsigned)(tag >> 32); ctl[C_PAR] = 1;
        if (lane == 0) st_agent(ws + WS_ACK + bid * 8, tag);
    } else {
        BAR();
        lds_wait_ge(ctl + C_PAR, 1);
        tag = ((u64)ctl[C_TAG_HI] << 32) | ctl[C_TAG_LO];
    }
    auto rx8 = __builtin_amdgcn_make_buffer_rsrc((void*)(ws + WS_X8), 0, M_ * K_, 0x00020000);
    const unsigned x8off = (mt * 8 + 2 * mw) * 16384 + lane * 16;
    v4i xa[16][2], bf[16][4], acc[2][4];
    auto ldx = [&](int u, int m) {
        xa[u][m] = __builtin_bit_cast(v4i, __builtin_amdgcn_raw_buffer_load_b128(rx8, x8off, ((4 * mt + u) & 15) * 1024 + m * 16384, SC1));
        FENCE();
    };
    auto ldb = [&](int u, int t) { bf[u][t] = *(const v4i*)(ldsb + u * 4096 + t * 1024 + lane * 16); FENCE(); };
#pragma unroll
    for (int t = 0; t < 8; ++t) acc[t >> 2][t & 3] = (v4i){0, 0, 0, 0};
    lds_wait_ge(ctl + C_OWN, 8);
    poll_flags(ws + WS_FLAG + (mt * 8 + 2 * mw + (lane & 1)) * FLAG_STRIDE, tag);
#pragma unroll
    for (int p = 0; p < 2 * XD; ++p) ldx(p >> 1, p & 1);
    v4f scv, ofv;
    unsigned rdy = 0;
    lds_wait_ge(ctl + C_GROUP, 4);
#pragma unroll
    for (int t = 0; t < 4; ++t) ldb(0, t);
#pragma unroll
    for (int u = 0; u < 16; ++u) {
#pragma unroll
        for (int k = 0; k < 8; ++k) {
            acc[k >> 2][k & 3] = __builtin_amdgcn_mfma_i32_16x16x64_i8(xa[u][k >> 2], bf[u][k & 3], acc[k >> 2][k & 3], 0, 0, 0);
            FENCE();
            if (k == 0 && u + 1 < 16 && ((u + 1) & 3) == 0) {
                if ((unsigned)__builtin_amdgcn_readfirstlane(rdy) < 4) lds_wait_ge(ctl + C_GROUP + ((u + 1) >> 2), 4);
                FENCE();
            }
            if (k < 4 && u + 1 < 16) ldb(u + 1, k);
            if ((k == 4 || k == 5) && u + XD < 16) ldx(u + XD, k - 4);
            if (k == 6 && u + 2 < 16 && ((u + 2) & 3) == 0) { rdy = ctl[C_GROUP + ((u + 2) >> 2)]; FENCE(); }
            if (k == 6 && u == 13) { rdy = ctl[C_EPI]; FENCE(); }
            if (k == 4 && u == 14) {
                if (__builtin_amdgcn_readfirstlane(rdy) == 0) lds_wait_ge(ctl + C_EPI, 1);
                FENCE();
                scv = *(const v4f*)(ldsb + LDS_EPI + 16 * c);
                ofv = *(const v4f*)(ldsb + LDS_EPI + 256 + 16 * c);
                FENCE();
            }
        }
    }
    auto ro = __builtin_amdgcn_make_buffer_rsrc((void*)out, 0, M_ * N_ * 2, 0x00020000);
    const unsigned ooff = ((mt * BM + 32 * mw + 4 * g) * N_ + n0 + 4 * c) * 2;
#pragma unroll
    for (int m = 0; m < 2; ++m)
#pragma unroll
        for (int r = 0; r < 4; ++r) {
            v2u o;
#pragma unroll
            for (int p = 0; p < 2; ++p) {
                const v2f v = (v2f){(float)acc[m][2 * p][r], (float)acc[m][2 * p + 1][r]} * (v2f){scv[2 * p], scv[2 * p + 1]}
                              + (v2f){ofv[2 * p], ofv[2 * p + 1]};
                o[p] = __builtin_bit_cast(unsigned, __builtin_convertvector(v, v2bf));
            }
            __builtin_amdgcn_raw_buffer_store_b64(o, ro, ooff, (16 * m + r) * N_ * 2, SC1);
        }
}

extern "C" void kernel_launch(void* const* d_in, const int* in_sizes, int n_in,
                              void* d_out, int out_size, void* d_ws, size_t ws_size,
                              hipStream_t stream) {
    w8a8_kernel<<<dim3(8, 4, 8), NT, 0, stream>>>((const int*)d_in[0], (const int*)d_in[1],
                                         (const unsigned short*)d_in[2], (const int*)d_in[3],
                                         (const unsigned short*)d_in[4], (const unsigned short*)d_in[5],
                                         (unsigned short*)d_out, (char*)d_ws);
}
```

```cpp
#include <hip/hip_runtime.h>
#include <hip/hip_bf16.h>
#include <stdint.h>

typedef int v4i __attribute__((ext_vector_type(4)));
typedef unsigned v4u __attribute__((ext_vector_type(4)));
typedef unsigned v2u __attribute__((ext_vector_type(2)));
typedef float v2f __attribute__((ext_vector_type(2)));
typedef float v4f __attribute__((ext_vector_type(4)));
typedef __bf16 v2bf __attribute__((ext_vector_type(2)));
typedef unsigned long long u64;
typedef __attribute__((address_space(1))) u64 gu64;
typedef __attribute__((address_space(3))) volatile unsigned lds_vu32;

constexpr int M_ = 512, K_ = 1024, N_ = 3584;
constexpr int BM = 128, BN = 64, NT = 12 * 64;
constexpr unsigned FLAG_STRIDE = 256, WS_FLAG = 4096, WS_ACK = 32768, WS_X8 = 262144;
constexpr u64 TAG_STEP = 0x9E3779B97F4A7C15ull;
constexpr int SC1 = 16;
constexpr unsigned SPIN_CAP = 1u << 15;
constexpr int LDS_CTL = 65536, LDS_PAR = LDS_CTL + 256, LDS_CS = LDS_PAR + 512, LDS_EPI = LDS_CS + 2048;
constexpr int LDS_BYTES = 84 * 1024;
enum { C_TAG_LO = 0, C_TAG_HI = 1, C_PAR = 2, C_OWN = 4, C_GROUP = 8, C_CSUM = 12, C_EPI = 13 };
constexpr unsigned CODE_HINT_BYTES = 10240;
constexpr int XD = 4;

#define PIN_ARGS() asm volatile("" :: "s"(qx), "s"(qw), "s"(sxp), "s"(zxp), "s"(swp), "s"(biasp), "s"(out), "s"(ws))
#define FENCE() do { asm volatile("" ::: "memory"); __builtin_amdgcn_sched_barrier(0); } while (0)
#define VMWAIT(n) do { asm volatile("s_waitcnt vmcnt(%0)" :: "i"(n) : "memory"); __builtin_amdgcn_sched_barrier(0); } while (0)
#define LGKM0() do { asm volatile("s_waitcnt lgkmcnt(0)" ::: "memory"); __builtin_amdgcn_sched_barrier(0); } while (0)
#define BAR() do { asm volatile("s_waitcnt lgkmcnt(0)\n\ts_barrier" ::: "memory"); __builtin_amdgcn_sched_barrier(0); } while (0)

__device__ __forceinline__ unsigned pack4(unsigned a, unsigned b, unsigned c, unsigned d) {
    return __builtin_amdgcn_perm(b, a, 0x0c0c0400u) | __builtin_amdgcn_perm(d, c, 0x04000c0cu);
}
__device__ __forceinline__ float rnd_bf16(float v) {
    const v2bf b = __builtin_convertvector((v2f){v, 0.f}, v2bf);
    return __builtin_bit_cast(float, __builtin_bit_cast(unsigned, b) << 16);
}
__device__ __forceinline__ float bf2f(unsigned short h) { return __builtin_bit_cast(float, (unsigned)h << 16); }
__device__ __forceinline__ u64 ld_agent(const void* p) { return __hip_atomic_load((gu64*)p, __ATOMIC_RELAXED, __HIP_MEMORY_SCOPE_AGENT); }
__device__ __forceinline__ void st_agent(void* p, u64 v) { __hip_atomic_store((gu64*)p, v, __ATOMIC_RELAXED, __HIP_MEMORY_SCOPE_AGENT); }
__device__ __forceinline__ void lds_wait_ge(lds_vu32* w, unsigned v) {
#pragma nounroll
    for (unsigned spin = 0; spin < (SPIN_CAP << 4); ++spin) {
        if ((unsigned)__builtin_amdgcn_readfirstlane(*w) >= v) break;
        __builtin_amdgcn_s_sleep(1);
    }
    FENCE();
}
__device__ __forceinline__ void poll_flags(const char* p, u64 tag) {
#pragma nounroll
    for (unsigned spin = 0; spin < SPIN_CAP; ++spin) {
        if (__all(ld_agent(p) == tag)) break;
        __builtin_amdgcn_s_sleep(8);
    }
    FENCE();
}

struct Loader {
    v4u wr[8][4], pk[4];
    int cs[4];
    __device__ __forceinline__ void packq(int i) {
#pragma unroll
        for (int t = 0; t < 4; ++t) {
            unsigned p = pack4(wr[i][0][t], wr[i][1][t], wr[i][2][t], wr[i][3][t]);
            cs[t] = __builtin_amdgcn_sdot4(p, 0x01010101, cs[t], false);
            asm volatile("" : "+v"(p), "+v"(cs[t]));
            pk[t][i & 3] = p;
        }
        FENCE();
    }
    __device__ __forceinline__ void stash(unsigned* lds, int slot, int lane) {
#pragma unroll
        for (int t = 0; t < 4; ++t) *(v4u*)((char*)lds + slot * 4096 + t * 1024 + lane * 16) = pk[t];
        LGKM0();
        if (lane == 0) atomicAdd(&lds[LDS_CTL / 4 + C_GROUP + (slot >> 2)], 1u);
        FENCE();
    }
    __device__ __forceinline__ void finish(unsigned* lds, int slot, int wv, int lane) {
        char* ldsb = (char*)lds;
        lds_vu32* ctl = (lds_vu32*)(lds + LDS_CTL / 4);
        packq(4); packq(5); packq(6); packq(7);
        stash(lds, slot, lane);
#pragma unroll
        for (int t = 0; t < 4; ++t) {
            auto r = __builtin_amdgcn_permlane32_swap(cs[t], cs[t], false, false);
            cs[t] = r[0] + r[1];
            r = __builtin_amdgcn_permlane16_swap(cs[t], cs[t], false, false);
            cs[t] = r[0] + r[1];
        }
        if (lane < 16) *(v4i*)(ldsb + LDS_CS + wv * 256 + lane * 16) = (v4i){cs[0], cs[1], cs[2], cs[3]};
        LGKM0();
        unsigned n = 0;
        if (lane == 0) n = atomicAdd(&lds[LDS_CTL / 4 + C_CSUM], 1u);
        if (__builtin_amdgcn_readfirstlane(n) == 7) {
            FENCE();
            int cst = 0;
#pragma unroll
            for (int k = 0; k < 8; ++k) cst += *(const int*)(ldsb + LDS_CS + k * 256 + lane * 4);
            lds_wait_ge(ctl + C_PAR, 1);
            const float sw = bf2f(*(const unsigned short*)(ldsb + LDS_PAR + 2 * lane));
            const float bi = bf2f(*(const unsigned short*)(ldsb + LDS_PAR + 128 + 2 * lane));
            const float sx = bf2f((unsigned short)lds[LDS_PAR / 4 + 64]);
            const float zc = rnd_bf16(rnd_bf16((float)cst) * rnd_bf16((float)(int)lds[LDS_PAR / 4 + 65]));
            const float sc = sw * sx;
            *(float*)(ldsb + LDS_EPI + lane * 4) = sc;
            *(float*)(ldsb + LDS_EPI + 256 + lane * 4) = bi - zc * sc;
            LGKM0();
            ctl[C_EPI] = 1;
        }
    }
};

__global__ __launch_bounds__(NT) __attribute__((aligned(4096)))
void w8a8_kernel(const int* __restrict__ qx, const int* __restrict__ qw,
                 const unsigned short* __restrict__ sxp, const int* __restrict__ zxp,
                 const unsigned short* __restrict__ swp, const unsigned short* __restrict__ biasp,
                 unsigned short* __restrict__ out, char* __restrict__ ws) {
    __shared__ __attribute__((aligned(16))) unsigned lds[LDS_BYTES / 4];
    u64 pc0;
    asm volatile("s_getpc_b64 %0" : "=s"(pc0));
    const int lane = threadIdx.x & 63;
    const int wv = __builtin_amdgcn_readfirstlane(threadIdx.x >> 6);
    const int grp = blockIdx.x, mt = blockIdx.y, bz = blockIdx.z;
    const int idx = bz * 4 + mt, bid = idx * 8 + grp;
    const int g = lane >> 4, c = lane & 15;
    char* ldsb = (char*)lds;
    lds_vu32* ctl = (lds_vu32*)(lds + LDS_CTL / 4);
    const bool conv = bz == 0;
    const int n0 = (grp * 7 + bz - 1) * BN;
    auto kstep_off = [&](int slot) { const int s = (4 * mt + slot) & 15; return (unsigned)((128 * (s >> 1) + 16 * (s & 1)) * (N_ * 4) + n0 * 4); };

    {
        if (__builtin_expect(wv < (conv ? 8 : 4), 1)) {
            Loader L;
            unsigned voff, xh = 0;
            if (conv) {
                voff = (lane >> 3) * (K_ * 4) + (lane & 7) * 16;
                unsigned so = (mt * 8 + grp) * (16 * K_ * 4) + wv * 512;
                asm volatile("" : "+s"(so));
                asm volatile("" : "+v"(voff));
                PIN_ARGS();
                auto rx = __builtin_amdgcn_make_buffer_rsrc((void*)qx, 0, M_ * K_ * 4, 0x00020000);
#pragma unroll
                for (int n = 0; n < 8; ++n) {
                    L.wr[n >> 2][n & 3] = __builtin_amdgcn_raw_buffer_load_b128(rx, voff + (n & 3) * 128, so + (n >> 2) * (8 * K_ * 4), 0);
                    FENCE();
                }
            } else {
                voff = g * (4 * N_ * 4) + c * 16;
                unsigned so = (256 * mt + 128 * (wv >> 1) + 16 * (wv & 1)) * (N_ * 4) + n0 * 4;
                asm volatile("" : "+s"(so));
                asm volatile("" : "+v"(voff));
                PIN_ARGS();
                auto rw = __builtin_amdgcn_make_buffer_rsrc((void*)qw, 0, K_ * N_ * 4, 0x00020000);
#pragma unroll
                for (int n = 0; n < 8; ++n) {
                    L.wr[n >> 2][n & 3] = __builtin_amdgcn_raw_buffer_load_b128(rw, voff, so + (32 * (n >> 2) + (n & 3)) * (N_ * 4), 0);
                    FENCE();
                }
                if (wv == 0) {
                    auto rx = __builtin_amdgcn_make_buffer_rsrc((void*)qx, 0, M_ * K_ * 4, 0x00020000);
                    unsigned hl = (bz - 1) * 37 + (lane < 36 ? lane : 36);
                    hl = hl < 255u ? hl : 255u;
                    xh = __builtin_amdgcn_raw_buffer_load_b32(rx, ((hl & 0xf0) << 8) | ((hl & 15) << 7), (mt * 8 + grp) * (16 * K_ * 4) + 2048, 0);
                    FENCE();
                }
            }
            asm volatile("" ::: "memory");
            if (conv) {
                const int r8 = lane >> 3, q8 = lane & 7;
                auto rx8 = __builtin_amdgcn_make_buffer_rsrc((void*)(ws + WS_X8), 0, M_ * K_, 0x00020000);
                char* xt = ldsb + wv * 2048;
#pragma unroll
                for (int hh = 0; hh < 2; ++hh) {
                    v4u p;
#pragma unroll
                    for (int i = 0; i < 4; ++i) { const v4u v = L.wr[hh][i]; p[i] = pack4(v[0], v[1], v[2], v[3]); }
                    *(v4u*)(xt + q8 * 256 + (((8 * hh + r8) ^ q8) * 16)) = p;
                }
#pragma unroll
                for (int s = 0; s < 2; ++s) {
                    const int q = 4 * s + g;
                    const v4u a = *(const v4u*)(xt + q * 256 + ((c ^ q) * 16));
                    __builtin_amdgcn_raw_buffer_store_b128(a, rx8, ((bid * 16 + 2 * wv + s) * 64 + lane) * 16, 0, SC1);
                }
                VMWAIT(0);
                BAR();
                return;
            }
            BAR();
            auto rw = __builtin_amdgcn_make_buffer_rsrc((void*)qw, 0, K_ * N_ * 4, 0x00020000);
            const unsigned soA = kstep_off(wv);
#pragma unroll
            for (int n = 8; n < 16; ++n) {
                L.wr[n >> 2][n & 3] = __builtin_amdgcn_raw_buffer_load_b128(rw, voff, soA + (32 * (n >> 2) + (n & 3)) * (N_ * 4), 0);
                FENCE();
            }
            L.cs[0] = L.cs[1] = L.cs[2] = L.cs[3] = 0;
            L.packq(0);
            VMWAIT(8);
            if (lane == 0) atomicAdd(&lds[LDS_CTL / 4 + C_OWN], 1u);
            L.packq(1); L.packq(2); L.packq(3);
            if (lane == 0) atomicAdd(&lds[LDS_CTL / 4 + C_OWN], 1u);
            L.stash(lds, wv, lane);
            asm volatile("" :: "v"(xh));
            const unsigned soB = kstep_off(wv + 4);
#pragma unroll
            for (int n = 0; n < 16; ++n) {
                L.wr[4 + (n >> 2)][n & 3] = __builtin_amdgcn_raw_buffer_load_b128(rw, voff, soB + (32 * (n >> 2) + (n & 3)) * (N_ * 4), 0);
                FENCE();
            }
            L.finish(lds, wv + 4, wv, lane);
            return;
        }
        PIN_ARGS();
        if (conv) {
            if (wv > 8) {
                BAR();
                const u64 base = pc0 & ~4095ull;
                const unsigned off = (wv - 9) * 4096 + lane * 64;
                if (off < CODE_HINT_BYTES) { const unsigned v = *(const __attribute__((address_space(1))) unsigned*)(base + off); asm volatile("" :: "v"(v)); }
            }
            if (wv != 8) return;
            const u64 tag = ld_agent(ws) + TAG_STEP;
            BAR();
            if (lane == 0) { st_agent(ws + WS_FLAG + bid * FLAG_STRIDE, tag); st_agent(ws + WS_ACK + bid * 8, tag); }
            if (bid == 0) {
                __builtin_amdgcn_s_sleep(60);
#pragma nounroll
                for (unsigned spin = 0; spin < SPIN_CAP; ++spin) {
                    bool ok = true;
#pragma unroll
                    for (int k = 0; k < 4; ++k) ok &= (ld_agent(ws + WS_ACK + (k * 64 + lane) * 8) == tag);
                    if (__all(ok)) break;
                    __builtin_amdgcn_s_sleep(8);
                }
                if (lane == 0) st_agent(ws, tag);
            }
            return;
        }
        if (wv < 8) {
            BAR();
            auto rw = __builtin_amdgcn_make_buffer_rsrc((void*)qw, 0, K_ * N_ * 4, 0x00020000);
            const unsigned soA = kstep_off(wv + 4), soB = kstep_off(wv + 8);
            const unsigned voff = g * (4 * N_ * 4) + c * 16;
            Loader L;
            L.cs[0] = L.cs[1] = L.cs[2] = L.cs[3] = 0;
            auto ld1 = [&](int n) {
                L.wr[n >> 2][n & 3] = __builtin_amdgcn_raw_buffer_load_b128(rw, voff, (n < 16 ? soA : soB) + (32 * ((n >> 2) & 3) + (n & 3)) * (N_ * 4), 0);
                FENCE();
            };
            lds_wait_ge(ctl + C_OWN, 4);
#pragma unroll
            for (int n = 0; n < 16; ++n) ld1(n);
#pragma unroll
            for (int n = 0; n < 16; ++n) {
                VMWAIT(15);
                ld1(n + 16);
                if ((n & 3) == 3) L.packq(n >> 2);
            }
            L.stash(lds, wv + 4, lane);
            L.finish(lds, wv + 8, wv, lane);
            return;
        }
    }

    const int mw = wv - 8;
    u64 tag;
    if (mw == 0) {
        tag = ld_agent(ws);
        const unsigned par = *(const unsigned*)((lane < 32 ? swp : biasp) + n0 + 2 * (lane & 31));
        const unsigned psx = __builtin_nontemporal_load(sxp);
        const unsigned pzx = __builtin_nontemporal_load(zxp);
        FENCE();
        lds[LDS_CTL / 4 + lane] = 0;
        BAR();
        tag += TAG_STEP;
        lds[LDS_PAR / 4 + lane] = par;
        lds[LDS_PAR / 4 + 64] = psx;
        lds[LDS_PAR / 4 + 65] = pzx;
        FENCE();
        ctl[C_TAG_LO] = (unsigned)tag; ctl[C_TAG_HI] = (unsigned)(tag >> 32); ctl[C_PAR] = 1;
        if (lane == 0) st_agent(ws + WS_ACK + bid * 8, tag);
    } else {
        BAR();
        lds_wait_ge(ctl + C_PAR, 1);
        tag = ((u64)ctl[C_TAG_HI] << 32) | ctl[C_TAG_LO];
    }
    auto rx8 = __builtin_amdgcn_make_buffer_rsrc((void*)(ws + WS_X8), 0, M_ * K_, 0x00020000);
    const unsigned x8off = (mt * 8 + 2 * mw) * 16384 + lane * 16;
    v4i xa[16][2], bf[16][4], acc[2][4];
    auto ldx = [&](int u, int m) {
        xa[u][m] = __builtin_bit_cast(v4i, __builtin_amdgcn_raw_buffer_load_b128(rx8, x8off, ((4 * mt + u) & 15) * 1024 + m * 16384, SC1));
        FENCE();
    };
    auto ldb = [&](int u, int t) { bf[u][t] = *(const v4i*)(ldsb + u * 4096 + t * 1024 + lane * 16); FENCE(); };
#pragma unroll
    for (int t = 0; t < 8; ++t) acc[t >> 2][t & 3] = (v4i){0, 0, 0, 0};
    lds_wait_ge(ctl + C_OWN, 8);
    poll_flags(ws + WS_FLAG + (mt * 8 + 2 * mw + (lane & 1)) * FLAG_STRIDE, tag);
#pragma unroll
    for (int p = 0; p < 2 * XD; ++p) ldx(p >> 1, p & 1);
    v4f scv, ofv;
    unsigned rdy = 0;
    lds_wait_ge(ctl + C_GROUP, 4);
#pragma unroll
    for (int t = 0; t < 4; ++t) ldb(0, t);
#pragma unroll
    for (int u = 0; u < 16; ++u) {
#pragma unroll
        for (int k = 0; k < 8; ++k) {
            acc[k >> 2][k & 3] = __builtin_amdgcn_mfma_i32_16x16x64_i8(xa[u][k >> 2], bf[u][k & 3], acc[k >> 2][k & 3], 0, 0, 0);
            FENCE();
            if (k == 0 && u + 1 < 16 && ((u + 1) & 3) == 0) {
                if ((unsigned)__builtin_amdgcn_readfirstlane(rdy) < 4) lds_wait_ge(ctl + C_GROUP + ((u + 1) >> 2), 4);
                FENCE();
            }
            if (k < 4 && u + 1 < 16) ldb(u + 1, k);
            if ((k == 4 || k == 5) && u + XD < 16) ldx(u + XD, k - 4);
            if (k == 6 && u + 2 < 16 && ((u + 2) & 3) == 0) { rdy = ctl[C_GROUP + ((u + 2) >> 2)]; FENCE(); }
            if (k == 6 && u == 13) { rdy = ctl[C_EPI]; FENCE(); }
            if (k == 4 && u == 14) {
                if (__builtin_amdgcn_readfirstlane(rdy) == 0) lds_wait_ge(ctl + C_EPI, 1);
                FENCE();
                scv = *(const v4f*)(ldsb + LDS_EPI + 16 * c);
                ofv = *(const v4f*)(ldsb + LDS_EPI + 256 + 16 * c);
                FENCE();
            }
        }
    }
    auto ro = __builtin_amdgcn_make_buffer_rsrc((void*)out, 0, M_ * N_ * 2, 0x00020000);
    const unsigned ooff = ((mt * BM + 32 * mw + 4 * g) * N_ + n0 + 4 * c) * 2;
#pragma unroll
    for (int m = 0; m < 2; ++m)
#pragma unroll
        for (int r = 0; r < 4; ++r) {
            v2u o;
#pragma unroll
            for (int p = 0; p < 2; ++p) {
                const v2f v = (v2f){(float)acc[m][2 * p][r], (float)acc[m][2 * p + 1][r]} * (v2f){scv[2 * p], scv[2 * p + 1]}
                              + (v2f){ofv[2 * p], ofv[2 * p + 1]};
                o[p] = __builtin_bit_cast(unsigned, __builtin_convertvector(v, v2bf));
            }
            __builtin_amdgcn_raw_buffer_store_b64(o, ro, ooff, (16 * m + r) * N_ * 2, SC1);
        }
}

extern "C" void kernel_launch(void* const* d_in, const int* in_sizes, int n_in,
                              void* d_out, int out_size, void* d_ws, size_t ws_size,
                              hipStream_t stream) {
    w8a8_kernel<<<dim3(8, 4, 8), NT, 0, stream>>>((const int*)d_in[0], (const int*)d_in[1],
                                         (const unsigned short*)d_in[2], (const int*)d_in[3],
                                         (const unsigned short*)d_in[4], (const unsigned short*)d_in[5],
                                         (unsigned short*)d_out, (char*)d_ws);
}
```

```cpp
#include <hip/hip_runtime.h>
#include <hip/hip_bf16.h>
#include <stdint.h>

typedef int v4i __attribute__((ext_vector_type(4)));
typedef unsigned v4u __attribute__((ext_vector_type(4)));
typedef unsigned v2u __attribute__((ext_vector_type(2)));
typedef float v2f __attribute__((ext_vector_type(2)));
typedef float v4f __attribute__((ext_vector_type(4)));
typedef __bf16 v2bf __attribute__((ext_vector_type(2)));
typedef unsigned long long u64;
typedef __attribute__((address_space(1))) u64 gu64;
typedef __attribute__((address_space(3))) volatile unsigned lds_vu32;

constexpr int M_ = 512, K_ = 1024, N_ = 3584;
constexpr int BM = 128, BN = 64, NT = 12 * 64;
constexpr unsigned FLAG_STRIDE = 256, WS_FLAG = 4096, WS_ACK = 32768, WS_X8 = 262144;
constexpr u64 TAG_STEP = 0x9E3779B97F4A7C15ull;
constexpr int SC1 = 16;
constexpr unsigned SPIN_CAP = 1u << 15;
constexpr int LDS_CTL = 65536, LDS_PAR = LDS_CTL + 256, LDS_CS = LDS_PAR + 512, LDS_EPI = LDS_CS + 2048;
constexpr int LDS_BYTES = 84 * 1024;
enum { C_TAG_LO = 0, C_TAG_HI = 1, C_PAR = 2, C_OWN = 4, C_GROUP = 8, C_CSUM = 12, C_EPI = 13 };
constexpr int XD = 4;

#define PIN_ARGS() asm volatile("" :: "s"(qx), "s"(qw), "s"(sxp), "s"(zxp), "s"(swp), "s"(biasp), "s"(out), "s"(ws))
#define FENCE() do { asm volatile("" ::: "memory"); __builtin_amdgcn_sched_barrier(0); } while (0)
#define VMWAIT(n) do { asm volatile("s_waitcnt vmcnt(%0)" :: "i"(n) : "memory"); __builtin_amdgcn_sched_barrier(0); } while (0)
#define LGKM0() do { asm volatile("s_waitcnt lgkmcnt(0)" ::: "memory"); __builtin_amdgcn_sched_barrier(0); } while (0)
#define BAR() do { asm volatile("s_waitcnt lgkmcnt(0)\n\ts_barrier" ::: "memory"); __builtin_amdgcn_sched_barrier(0); } while (0)

__device__ __forceinline__ unsigned pack4(unsigned a, unsigned b, unsigned c, unsigned d) {
    return __builtin_amdgcn_perm(b, a, 0x0c0c0400u) | __builtin_amdgcn_perm(d, c, 0x04000c0cu);
}
__device__ __forceinline__ float rnd_bf16(float v) {
    const v2bf b = __builtin_convertvector((v2f){v, 0.f}, v2bf);
    return __builtin_bit_cast(float, __builtin_bit_cast(unsigned, b) << 16);
}
__device__ __forceinline__ float bf2f(unsigned short h) { return __builtin_bit_cast(float, (unsigned)h << 16); }
__device__ __forceinline__ u64 ld_agent(const void* p) { return __hip_atomic_load((gu64*)p, __ATOMIC_RELAXED, __HIP_MEMORY_SCOPE_AGENT); }
__device__ __forceinline__ void st_agent(void* p, u64 v) { __hip_atomic_store((gu64*)p, v, __ATOMIC_RELAXED, __HIP_MEMORY_SCOPE_AGENT); }
__device__ __forceinline__ void lds_wait_ge(lds_vu32* w, unsigned v) {
#pragma nounroll
    for (unsigned spin = 0; spin < (SPIN_CAP << 4); ++spin) {
        if ((unsigned)__builtin_amdgcn_readfirstlane(*w) >= v) break;
        __builtin_amdgcn_s_sleep(1);
    }
    FENCE();
}
__device__ __forceinline__ void poll_flags(const char* p, u64 tag) {
#pragma nounroll
    for (unsigned spin = 0; spin < SPIN_CAP; ++spin) {
        if (__all(ld_agent(p) == tag)) break;
        __builtin_amdgcn_s_sleep(8);
    }
    FENCE();
}

struct Loader {
    v4u wr[8][4], pk[4];
    int cs[4];
    __device__ __forceinline__ void packq(int i) {
#pragma unroll
        for (int t = 0; t < 4; ++t) {
            unsigned p = pack4(wr[i][0][t], wr[i][1][t], wr[i][2][t], wr[i][3][t]);
            cs[t] = __builtin_amdgcn_sdot4(p, 0x01010101, cs[t], false);
            asm volatile("" : "+v"(p), "+v"(cs[t]));
            pk[t][i & 3] = p;
        }
        FENCE();
    }
    __device__ __forceinline__ void stash(unsigned* lds, int slot, int lane) {
#pragma unroll
        for (int t = 0; t < 4; ++t) *(v4u*)((char*)lds + slot * 4096 + t * 1024 + lane * 16) = pk[t];
        LGKM0();
        if (lane == 0) atomicAdd(&lds[LDS_CTL / 4 + C_GROUP + (slot >> 2)], 1u);
        FENCE();
    }
    __device__ __forceinline__ void finish(unsigned* lds, int slot, int wv, int lane) {
        char* ldsb = (char*)lds;
        lds_vu32* ctl = (lds_vu32*)(lds + LDS_CTL / 4);
        packq(4); packq(5); packq(6); packq(7);
        stash(lds, slot, lane);
#pragma unroll
        for (int t = 0; t < 4; ++t) {
            auto r = __builtin_amdgcn_permlane32_swap(cs[t], cs[t], false, false);
            cs[t] = r[0] + r[1];
            r = __builtin_amdgcn_permlane16_swap(cs[t], cs[t], false, false);
            cs[t] = r[0] + r[1];
        }
        if (lane < 16) *(v4i*)(ldsb + LDS_CS + wv * 256 + lane * 16) = (v4i){cs[0], cs[1], cs[2], cs[3]};
        LGKM0();
        unsigned n = 0;
        if (lane == 0) n = atomicAdd(&lds[LDS_CTL / 4 + C_CSUM], 1u);
        if (__builtin_amdgcn_readfirstlane(n) == 7) {
            FENCE();
            int cst = 0;
#pragma unroll
            for (int k = 0; k < 8; ++k) cst += *(const int*)(ldsb + LDS_CS + k * 256 + lane * 4);
            lds_wait_ge(ctl + C_PAR, 1);
            const float sw = bf2f(*(const unsigned short*)(ldsb + LDS_PAR + 2 * lane));
            const float bi = bf2f(*(const unsigned short*)(ldsb + LDS_PAR + 128 + 2 * lane));
            const float sx = bf2f((unsigned short)lds[LDS_PAR / 4 + 64]);
            const float zc = rnd_bf16(rnd_bf16((float)cst) * rnd_bf16((float)(int)lds[LDS_PAR / 4 + 65]));
            const float sc = sw * sx;
            *(float*)(ldsb + LDS_EPI + lane * 4) = sc;
            *(float*)(ldsb + LDS_EPI + 256 + lane * 4) = bi - zc * sc;
            LGKM0();
            ctl[C_EPI] = 1;
        }
    }
};

__global__ __launch_bounds__(NT) __attribute__((aligned(4096)))
void w8a8_kernel(const int* __restrict__ qx, const int* __restrict__ qw,
                 const unsigned short* __restrict__ sxp, const int* __restrict__ zxp,
                 const unsigned short* __restrict__ swp, const unsigned short* __restrict__ biasp,
                 unsigned short* __restrict__ out, char* __restrict__ ws) {
    __shared__ __attribute__((aligned(16))) unsigned lds[LDS_BYTES / 4];
    const int lane = threadIdx.x & 63;
    const int wv = __builtin_amdgcn_readfirstlane(threadIdx.x >> 6);
    const int grp = blockIdx.x, mt = blockIdx.y, bz = blockIdx.z;
    const int idx = bz * 4 + mt, bid = idx * 8 + grp;
    const int g = lane >> 4, c = lane & 15;
    char* ldsb = (char*)lds;
    lds_vu32* ctl = (lds_vu32*)(lds + LDS_CTL / 4);
    const bool conv = bz == 0;
    const int n0 = (grp * 7 + bz - 1) * BN;
    auto kstep_off = [&](int slot) { const int s = (4 * mt + slot) & 15; return (unsigned)((128 * (s >> 1) + 16 * (s & 1)) * (N_ * 4) + n0 * 4); };

    {
        if (__builtin_expect(wv < (conv ? 8 : 4), 1)) {
            Loader L;
            unsigned voff, xh = 0;
            if (conv) {
                voff = (lane >> 3) * (K_ * 4) + (lane & 7) * 16;
                unsigned so = (mt * 8 + grp) * (16 * K_ * 4) + wv * 512;
                asm volatile("" : "+s"(so));
                asm volatile("" : "+v"(voff));
                PIN_ARGS();
                auto rx = __builtin_amdgcn_make_buffer_rsrc((void*)qx, 0, M_ * K_ * 4, 0x00020000);
#pragma unroll
                for (int n = 0; n < 8; ++n) {
                    L.wr[n >> 2][n & 3] = __builtin_amdgcn_raw_buffer_load_b128(rx, voff + (n & 3) * 128, so + (n >> 2) * (8 * K_ * 4), 0);
                    FENCE();
                }
            } else {
                voff = g * (4 * N_ * 4) + c * 16;
                unsigned so = (256 * mt + 128 * (wv >> 1) + 16 * (wv & 1)) * (N_ * 4) + n0 * 4;
                asm volatile("" : "+s"(so));
                asm volatile("" : "+v"(voff));
                PIN_ARGS();
                auto rw = __builtin_amdgcn_make_buffer_rsrc((void*)qw, 0, K_ * N_ * 4, 0x00020000);
#pragma unroll
                for (int n = 0; n < 8; ++n) {
                    L.wr[n >> 2][n & 3] = __builtin_amdgcn_raw_buffer_load_b128(rw, voff, so + (32 * (n >> 2) + (n & 3)) * (N_ * 4), 0);
                    FENCE();
                }
                if (wv == 0) {
                    auto rx = __builtin_amdgcn_make_buffer_rsrc((void*)qx, 0, M_ * K_ * 4, 0x00020000);
                    unsigned hl = (bz - 1) * 37 + (lane < 36 ? lane : 36);
                    hl = hl < 255u ? hl : 255u;
                    xh = __builtin_amdgcn_raw_buffer_load_b32(rx, ((hl & 0xf0) << 8) | ((hl & 15) << 7), (mt * 8 + grp) * (16 * K_ * 4) + 2048, 0);
                    FENCE();
                }
            }
            asm volatile("" ::: "memory");
            if (conv) {
                const int r8 = lane >> 3, q8 = lane & 7;
                auto rx8 = __builtin_amdgcn_make_buffer_rsrc((void*)(ws + WS_X8), 0, M_ * K_, 0x00020000);
                char* xt = ldsb + wv * 2048;
#pragma unroll
                for (int hh = 0; hh < 2; ++hh) {
                    v4u p;
#pragma unroll
                    for (int i = 0; i < 4; ++i) { const v4u v = L.wr[hh][i]; p[i] = pack4(v[0], v[1], v[2], v[3]); }
                    *(v4u*)(xt + q8 * 256 + (((8 * hh + r8) ^ q8) * 16)) = p;
                }
#pragma unroll
                for (int s = 0; s < 2; ++s) {
                    const int q = 4 * s + g;
                    const v4u a = *(const v4u*)(xt + q * 256 + ((c ^ q) * 16));
                    __builtin_amdgcn_raw_buffer_store_b128(a, rx8, ((bid * 16 + 2 * wv + s) * 64 + lane) * 16, 0, SC1);
                }
                VMWAIT(0);
                BAR();
                return;
            }
            BAR();
            auto rw = __builtin_amdgcn_make_buffer_rsrc((void*)qw, 0, K_ * N_ * 4, 0x00020000);
            const unsigned soA = kstep_off(wv);
#pragma unroll
            for (int n = 8; n < 16; ++n) {
                L.wr[n >> 2][n & 3] = __builtin_amdgcn_raw_buffer_load_b128(rw, voff, soA + (32 * (n >> 2) + (n & 3)) * (N_ * 4), 0);
                FENCE();
            }
            L.cs[0] = L.cs[1] = L.cs[2] = L.cs[3] = 0;
            L.packq(0);
            VMWAIT(8);
            if (lane == 0) atomicAdd(&lds[LDS_CTL / 4 + C_OWN], 1u);
            L.packq(1); L.packq(2); L.packq(3);
            if (lane == 0) atomicAdd(&lds[LDS_CTL / 4 + C_OWN], 1u);
            L.stash(lds, wv, lane);
            asm volatile("" :: "v"(xh));
            const unsigned soB = kstep_off(wv + 4);
#pragma unroll
            for (int n = 0; n < 16; ++n) {
                L.wr[4 + (n >> 2)][n & 3] = __builtin_amdgcn_raw_buffer_load_b128(rw, voff, soB + (32 * (n >> 2) + (n & 3)) * (N_ * 4), 0);
                FENCE();
            }
            L.finish(lds, wv + 4, wv, lane);
            return;
        }
        PIN_ARGS();
        if (conv) {
            if (wv != 8) return;
            const u64 tag = ld_agent(ws) + TAG_STEP;
            BAR();
            if (lane == 0) { st_agent(ws + WS_FLAG + bid * FLAG_STRIDE, tag); st_agent(ws + WS_ACK + bid * 8, tag); }
            if (bid == 0) {
                __builtin_amdgcn_s_sleep(60);
#pragma nounroll
                for (unsigned spin = 0; spin < SPIN_CAP; ++spin) {
                    bool ok = true;
#pragma unroll
                    for (int k = 0; k < 4; ++k) ok &= (ld_agent(ws + WS_ACK + (k * 64 + lane) * 8) == tag);
                    if (__all(ok)) break;
                    __builtin_amdgcn_s_sleep(8);
                }
                if (lane == 0) st_agent(ws, tag);
            }
            return;
        }
        if (wv < 8) {
            BAR();
            auto rw = __builtin_amdgcn_make_buffer_rsrc((void*)qw, 0, K_ * N_ * 4, 0x00020000);
            const unsigned soA = kstep_off(wv + 4), soB = kstep_off(wv + 8);
            const unsigned voff = g * (4 * N_ * 4) + c * 16;
            Loader L;
            L.cs[0] = L.cs[1] = L.cs[2] = L.cs[3] = 0;
            auto ld1 = [&](int n) {
                L.wr[n >> 2][n & 3] = __builtin_amdgcn_raw_buffer_load_b128(rw, voff, (n < 16 ? soA : soB) + (32 * ((n >> 2) & 3) + (n & 3)) * (N_ * 4), 0);
                FENCE();
            };
            lds_wait_ge(ctl + C_OWN, 4);
#pragma unroll
            for (int n = 0; n < 16; ++n) ld1(n);
#pragma unroll
            for (int n = 0; n < 16; ++n) {
                VMWAIT(15);
                ld1(n + 16);
                if ((n & 3) == 3) L.packq(n >> 2);
            }
            L.stash(lds, wv + 4, lane);
            L.finish(lds, wv + 8, wv, lane);
            return;
        }
    }

    const int mw = wv - 8;
    u64 tag;
    if (mw == 0) {
        tag = ld_agent(ws);
        const unsigned par = *(const unsigned*)((lane < 32 ? swp : biasp) + n0 + 2 * (lane & 31));
        const unsigned psx = __builtin_nontemporal_load(sxp);
        const unsigned pzx = __builtin_nontemporal_load(zxp);
        FENCE();
        lds[LDS_CTL / 4 + lane] = 0;
        BAR();
        tag += TAG_STEP;
        lds[LDS_PAR / 4 + lane] = par;
        lds[LDS_PAR / 4 + 64] = psx;
        lds[LDS_PAR / 4 + 65] = pzx;
        FENCE();
        ctl[C_TAG_LO] = (unsigned)tag; ctl[C_TAG_HI] = (unsigned)(tag >> 32); ctl[C_PAR] = 1;
        if (lane == 0) st_agent(ws + WS_ACK + bid * 8, tag);
    } else {
        BAR();
        lds_wait_ge(ctl + C_PAR, 1);
        tag = ((u64)ctl[C_TAG_HI] << 32) | ctl[C_TAG_LO];
    }
    auto rx8 = __builtin_amdgcn_make_buffer_rsrc((void*)(ws + WS_X8), 0, M_ * K_, 0x00020000);
    const unsigned x8off = (mt * 8 + 2 * mw) * 16384 + lane * 16;
    v4i xa[16][2], bf[16][4], acc[2][4];
    auto ldx = [&](int u, int m) {
        xa[u][m] = __builtin_bit_cast(v4i, __builtin_amdgcn_raw_buffer_load_b128(rx8, x8off, ((4 * mt + u) & 15) * 1024 + m * 16384, SC1));
        FENCE();
    };
    auto ldb = [&](int u, int t) { bf[u][t] = *(const v4i*)(ldsb + u * 4096 + t * 1024 + lane * 16); FENCE(); };
#pragma unroll
    for (int t = 0; t < 8; ++t) acc[t >> 2][t & 3] = (v4i){0, 0, 0, 0};
    lds_wait_ge(ctl + C_OWN, 8);
    poll_flags(ws + WS_FLAG + (mt * 8 + 2 * mw + (lane & 1)) * FLAG_STRIDE, tag);
#pragma unroll
    for (int p = 0; p < 2 * XD; ++p) ldx(p >> 1, p & 1);
    v4f scv, ofv;
    unsigned rdy = 0;
    lds_wait_ge(ctl + C_GROUP, 4);
#pragma unroll
    for (int t = 0; t < 4; ++t) ldb(0, t);
#pragma unroll
    for (int u = 0; u < 16; ++u) {
#pragma unroll
        for (int k = 0; k < 8; ++k) {
            acc[k >> 2][k & 3] = __builtin_amdgcn_mfma_i32_16x16x64_i8(xa[u][k >> 2], bf[u][k & 3], acc[k >> 2][k & 3], 0, 0, 0);
            FENCE();
            if (k == 0 && u + 1 < 16 && ((u + 1) & 3) == 0) {
                if ((unsigned)__builtin_amdgcn_readfirstlane(rdy) < 4) lds_wait_ge(ctl + C_GROUP + ((u + 1) >> 2), 4);
                FENCE();
            }
            if (k < 4 && u + 1 < 16) ldb(u + 1, k);
            if ((k == 4 || k == 5) && u + XD < 16) ldx(u + XD, k - 4);
            if (k == 6 && u + 2 < 16 && ((u + 2) & 3) == 0) { rdy = ctl[C_GROUP + ((u + 2) >> 2)]; FENCE(); }
            if (k == 6 && u == 13) { rdy = ctl[C_EPI]; FENCE(); }
            if (k == 4 && u == 14) {
                if (__builtin_amdgcn_readfirstlane(rdy) == 0) lds_wait_ge(ctl + C_EPI, 1);
                FENCE();
                scv = *(const v4f*)(ldsb + LDS_EPI + 16 * c);
                ofv = *(const v4f*)(ldsb + LDS_EPI + 256 + 16 * c);
                FENCE();
            }
        }
    }
    auto ro = __builtin_amdgcn_make_buffer_rsrc((void*)out, 0, M_ * N_ * 2, 0x00020000);
    const unsigned ooff = ((mt * BM + 32 * mw + 4 * g) * N_ + n0 + 4 * c) * 2;
#pragma unroll
    for (int m = 0; m < 2; ++m)
#pragma unroll
        for (int r = 0; r < 4; ++r) {
            v2u o;
#pragma unroll
            for (int p = 0; p < 2; ++p) {
                const v2f v = (v2f){(float)acc[m][2 * p][r], (float)acc[m][2 * p + 1][r]} * (v2f){scv[2 * p], scv[2 * p + 1]}
                              + (v2f){ofv[2 * p], ofv[2 * p + 1]};
                o[p] = __builtin_bit_cast(unsigned, __builtin_convertvector(v, v2bf));
            }
            __builtin_amdgcn_raw_buffer_store_b64(o, ro, ooff, (16 * m + r) * N_ * 2, SC1);
        }
}

extern "C" void kernel_launch(void* const* d_in, const int* in_sizes, int n_in,
                              void* d_out, int out_size, void* d_ws, size_t ws_size,
                              hipStream_t stream) {
    w8a8_kernel<<<dim3(8, 4, 8), NT, 0, stream>>>((const int*)d_in[0], (const int*)d_in[1],
                                         (const unsigned short*)d_in[2], (const int*)d_in[3],
                                         (const unsigned short*)d_in[4], (const unsigned short*)d_in[5],
                                         (unsigned short*)d_out, (char*)d_ws);
}
```

```cpp
#include <hip/hip_runtime.h>
#include <hip/hip_bf16.h>
#include <stdint.h>

typedef int v4i __attribute__((ext_vector_type(4)));
typedef unsigned v4u __attribute__((ext_vector_type(4)));
typedef unsigned v2u __attribute__((ext_vector_type(2)));
typedef float v2f __attribute__((ext_vector_type(2)));
typedef float v4f __attribute__((ext_vector_type(4)));
typedef __bf16 v2bf __attribute__((ext_vector_type(2)));
typedef unsigned long long u64;
typedef __attribute__((address_space(1))) u64 gu64;
typedef __attribute__((address_space(3))) volatile unsigned lds_vu32;

constexpr int M_ = 512, K_ = 1024, N_ = 3584;
constexpr int BM = 128, BN = 64, NT = 12 * 64;
constexpr unsigned FLAG_STRIDE = 256, WS_FLAG = 4096, WS_ACK = 32768, WS_X8 = 262144;
constexpr u64 TAG_STEP = 0x9E3779B97F4A7C15ull;
constexpr int SC1 = 16;
constexpr unsigned SPIN_CAP = 1u << 15;
constexpr int LDS_CTL = 65536, LDS_PAR = LDS_CTL + 256, LDS_CS = LDS_PAR + 512, LDS_EPI = LDS_CS + 2048;
constexpr int LDS_BYTES = 84 * 1024;
enum { C_TAG_LO = 0, C_TAG_HI = 1, C_PAR = 2, C_OWN = 4, C_GROUP = 8, C_CSUM = 12, C_EPI = 13 };
constexpr int XD = 4;

#define PIN_ARGS() asm volatile("" :: "s"(qx), "s"(qw), "s"(sxp), "s"(zxp), "s"(swp), "s"(biasp), "s"(out), "s"(ws))
#define FENCE() do { asm volatile("" ::: "memory"); __builtin_amdgcn_sched_barrier(0); } while (0)
#define VMWAIT(n) do { asm volatile("s_waitcnt vmcnt(%0)" :: "i"(n) : "memory"); __builtin_amdgcn_sched_barrier(0); } while (0)
#define LGKM0() do { asm volatile("s_waitcnt lgkmcnt(0)" ::: "memory"); __builtin_amdgcn_sched_barrier(0); } while (0)
#define BAR() do { asm volatile("s_waitcnt lgkmcnt(0)\n\ts_barrier" ::: "memory"); __builtin_amdgcn_sched_barrier(0); } while (0)

__device__ __forceinline__ unsigned pack4(unsigned a, unsigned b, unsigned c, unsigned d) {
    return __builtin_amdgcn_perm(b, a, 0x0c0c0400u) | __builtin_amdgcn_perm(d, c, 0x04000c0cu);
}
__device__ __forceinline__ float rnd_bf16(float v) {
    const v2bf b = __builtin_convertvector((v2f){v, 0.f}, v2bf);
    return __builtin_bit_cast(float, __builtin_bit_cast(unsigned, b) << 16);
}
__device__ __forceinline__ float bf2f(unsigned short h) { return __builtin_bit_cast(float, (unsigned)h << 16); }
__device__ __forceinline__ u64 ld_agent(const void* p) { return __hip_atomic_load((gu64*)p, __ATOMIC_RELAXED, __HIP_MEMORY_SCOPE_AGENT); }
__device__ __forceinline__ void st_agent(void* p, u64 v) { __hip_atomic_store((gu64*)p, v, __ATOMIC_RELAXED, __HIP_MEMORY_SCOPE_AGENT); }
__device__ __forceinline__ void lds_wait_ge(lds_vu32* w, unsigned v) {
#pragma nounroll
    for (unsigned spin = 0; spin < (SPIN_CAP << 4); ++spin) {
        if ((unsigned)__builtin_amdgcn_readfirstlane(*w) >= v) break;
        __builtin_amdgcn_s_sleep(1);
    }
    FENCE();
}
__device__ __forceinline__ void poll_flags(const char* p, u64 tag) {
#pragma nounroll
    for (unsigned spin = 0; spin < SPIN_CAP; ++spin) {
        if (__all(ld_agent(p) == tag)) break;
        __builtin_amdgcn_s_sleep(8);
    }
    FENCE();
}

struct Loader {
    v4u wr[8][4], pk[4];
    int cs[4];
    __device__ __forceinline__ void packq(int i) {
#pragma unroll
        for (int t = 0; t < 4; ++t) {
            unsigned p = pack4(wr[i][0][t], wr[i][1][t], wr[i][2][t], wr[i][3][t]);
            cs[t] = __builtin_amdgcn_sdot4(p, 0x01010101, cs[t], false);
            asm volatile("" : "+v"(p), "+v"(cs[t]));
            pk[t][i & 3] = p;
        }
        FENCE();
    }
    __device__ __forceinline__ void stash(unsigned* lds, int slot, int lane) {
#pragma unroll
        for (int t = 0; t < 4; ++t) *(v4u*)((char*)lds + slot * 4096 + t * 1024 + lane * 16) = pk[t];
        LGKM0();
        if (lane == 0) atomicAdd(&lds[LDS_CTL / 4 + C_GROUP + (slot >> 2)], 1u);
        FENCE();
    }
    __device__ __forceinline__ void finish(unsigned* lds, int slot, int wv, int lane) {
        char* ldsb = (char*)lds;
        lds_vu32* ctl = (lds_vu32*)(lds + LDS_CTL / 4);
        packq(4); packq(5); packq(6); packq(7);
        stash(lds, slot, lane);
#pragma unroll
        for (int t = 0; t < 4; ++t) {
            auto r = __builtin_amdgcn_permlane32_swap(cs[t], cs[t], false, false);
            cs[t] = r[0] + r[1];
            r = __builtin_amdgcn_permlane16_swap(cs[t], cs[t], false, false);
            cs[t] = r[0] + r[1];
        }
        if (lane < 16) *(v4i*)(ldsb + LDS_CS + wv * 256 + lane * 16) = (v4i){cs[0], cs[1], cs[2], cs[3]};
        LGKM0();
        unsigned n = 0;
        if (lane == 0) n = atomicAdd(&lds[LDS_CTL / 4 + C_CSUM], 1u);
        if (__builtin_amdgcn_readfirstlane(n) == 7) {
            FENCE();
            int cst = 0;
#pragma unroll
            for (int k = 0; k < 8; ++k) cst += *(const int*)(ldsb + LDS_CS + k * 256 + lane * 4);
            lds_wait_ge(ctl + C_PAR, 1);
            const float sw = bf2f(*(const unsigned short*)(ldsb + LDS_PAR + 2 * lane));
            const float bi = bf2f(*(const unsigned short*)(ldsb + LDS_PAR + 128 + 2 * lane));
            const float sx = bf2f((unsigned short)lds[LDS_PAR / 4 + 64]);
            const float zc = rnd_bf16(rnd_bf16((float)cst) * rnd_bf16((float)(int)lds[LDS_PAR / 4 + 65]));
            const float sc = sw * sx;
            *(float*)(ldsb + LDS_EPI + lane * 4) = sc;
            *(float*)(ldsb + LDS_EPI + 256 + lane * 4) = bi - zc * sc;
            LGKM0();
            ctl[C_EPI] = 1;
        }
    }
};

__global__ __launch_bounds__(NT) __attribute__((aligned(4096)))
void w8a8_kernel(const int* __restrict__ qx, const int* __restrict__ qw,
                 const unsigned short* __restrict__ sxp, const int* __restrict__ zxp,
                 const unsigned short* __restrict__ swp, const unsigned short* __restrict__ biasp,
                 unsigned short* __restrict__ out, char* __restrict__ ws) {
    __shared__ __attribute__((aligned(16))) unsigned lds[LDS_BYTES / 4];
    const int wv = __builtin_amdgcn_readfirstlane(threadIdx.x >> 6);
    const bool conv = blockIdx.z == 0;
    char* ldsb = (char*)lds;
    lds_vu32* ctl = (lds_vu32*)(lds + LDS_CTL / 4);
#define IDS() \
    int tid_ = threadIdx.x, grp = blockIdx.x, mt = blockIdx.y, bz = blockIdx.z; \
    asm volatile("" : "+v"(tid_)); \
    asm volatile("" : "+s"(grp), "+s"(mt), "+s"(bz)); \
    const int lane = tid_ & 63, g = lane >> 4, c = lane & 15; \
    const int bid = (bz * 4 + mt) * 8 + grp, n0 = (grp * 7 + bz - 1) * BN; \
    auto kstep_off = [&](int slot) { const int s = (4 * mt + slot) & 15; return (unsigned)((128 * (s >> 1) + 16 * (s & 1)) * (N_ * 4) + n0 * 4); }; \
    (void)g; (void)c; (void)bid; (void)kstep_off

    {
        const int ncrit = conv ? 8 : 4;
        asm volatile("s_cmp_lt_u32 %0, %1\n\ts_cbranch_scc1 1f\n\ts_sleep 4\n1:" :: "s"(wv), "s"(ncrit) : "scc");
        if (__builtin_expect(wv < ncrit, 1)) {
            IDS();
            Loader L;
            unsigned voff, xh = 0;
            if (conv) {
                voff = (lane >> 3) * (K_ * 4) + (lane & 7) * 16;
                unsigned so = (mt * 8 + grp) * (16 * K_ * 4) + wv * 512;
                asm volatile("" : "+s"(so));
                asm volatile("" : "+v"(voff));
                PIN_ARGS();
                auto rx = __builtin_amdgcn_make_buffer_rsrc((void*)qx, 0, M_ * K_ * 4, 0x00020000);
#pragma unroll
                for (int n = 0; n < 8; ++n) {
                    L.wr[n >> 2][n & 3] = __builtin_amdgcn_raw_buffer_load_b128(rx, voff + (n & 3) * 128, so + (n >> 2) * (8 * K_ * 4), 0);
                    FENCE();
                }
            } else {
                voff = g * (4 * N_ * 4) + c * 16;
                unsigned so = (256 * mt + 128 * (wv >> 1) + 16 * (wv & 1)) * (N_ * 4) + n0 * 4;
                asm volatile("" : "+s"(so));
                asm volatile("" : "+v"(voff));
                PIN_ARGS();
                auto rw = __builtin_amdgcn_make_buffer_rsrc((void*)qw, 0, K_ * N_ * 4, 0x00020000);
#pragma unroll
                for (int n = 0; n < 8; ++n) {
                    L.wr[n >> 2][n & 3] = __builtin_amdgcn_raw_buffer_load_b128(rw, voff, so + (32 * (n >> 2) + (n & 3)) * (N_ * 4), 0);
                    FENCE();
                }
                if (wv == 0) {
                    auto rx = __builtin_amdgcn_make_buffer_rsrc((void*)qx, 0, M_ * K_ * 4, 0x00020000);
                    unsigned hl = (bz - 1) * 37 + (lane < 36 ? lane : 36);
                    hl = hl < 255u ? hl : 255u;
                    xh = __builtin_amdgcn_raw_buffer_load_b32(rx, ((hl & 0xf0) << 8) | ((hl & 15) << 7), (mt * 8 + grp) * (16 * K_ * 4) + 2048, 0);
                    FENCE();
                }
            }
            asm volatile("" ::: "memory");
            if (conv) {
                const int r8 = lane >> 3, q8 = lane & 7;
                auto rx8 = __builtin_amdgcn_make_buffer_rsrc((void*)(ws + WS_X8), 0, M_ * K_, 0x00020000);
                char* xt = ldsb + wv * 2048;
#pragma unroll
                for (int hh = 0; hh < 2; ++hh) {
                    v4u p;
#pragma unroll
                    for (int i = 0; i < 4; ++i) { const v4u v = L.wr[hh][i]; p[i] = pack4(v[0], v[1], v[2], v[3]); }
                    *(v4u*)(xt + q8 * 256 + (((8 * hh + r8) ^ q8) * 16)) = p;
                }
#pragma unroll
                for (int s = 0; s < 2; ++s) {
                    const int q = 4 * s + g;
                    const v4u a = *(const v4u*)(xt + q * 256 + ((c ^ q) * 16));
                    __builtin_amdgcn_raw_buffer_store_b128(a, rx8, ((bid * 16 + 2 * wv + s) * 64 + lane) * 16, 0, SC1);
                }
                VMWAIT(0);
                BAR();
                return;
            }
            BAR();
            auto rw = __builtin_amdgcn_make_buffer_rsrc((void*)qw, 0, K_ * N_ * 4, 0x00020000);
            const unsigned soA = kstep_off(wv);
#pragma unroll
            for (int n = 8; n < 16; ++n) {
                L.wr[n >> 2][n & 3] = __builtin_amdgcn_raw_buffer_load_b128(rw, voff, soA + (32 * (n >> 2) + (n & 3)) * (N_ * 4), 0);
                FENCE();
            }
            L.cs[0] = L.cs[1] = L.cs[2] = L.cs[3] = 0;
            L.packq(0);
            VMWAIT(8);
            if (lane == 0) atomicAdd(&lds[LDS_CTL / 4 + C_OWN], 1u);
            L.packq(1); L.packq(2); L.packq(3);
            if (lane == 0) atomicAdd(&lds[LDS_CTL / 4 + C_OWN], 1u);
            L.stash(lds, wv, lane);
            asm volatile("" :: "v"(xh));
            const unsigned soB = kstep_off(wv + 4);
#pragma unroll
            for (int n = 0; n < 16; ++n) {
                L.wr[4 + (n >> 2)][n & 3] = __builtin_amdgcn_raw_buffer_load_b128(rw, voff, soB + (32 * (n >> 2) + (n & 3)) * (N_ * 4), 0);
                FENCE();
            }
            L.finish(lds, wv + 4, wv, lane);
            return;
        }
        PIN_ARGS();
        if (conv) {
            if (wv != 8) return;
            IDS();
            const u64 tag = ld_agent(ws) + TAG_STEP;
            BAR();
            if (lane == 0) { st_agent(ws + WS_FLAG + bid * FLAG_STRIDE, tag); st_agent(ws + WS_ACK + bid * 8, tag); }
            if (bid == 0) {
                __builtin_amdgcn_s_sleep(60);
#pragma nounroll
                for (unsigned spin = 0; spin < SPIN_CAP; ++spin) {
                    bool ok = true;
#pragma unroll
                    for (int k = 0; k < 4; ++k) ok &= (ld_agent(ws + WS_ACK + (k * 64 + lane) * 8) == tag);
                    if (__all(ok)) break;
                    __builtin_amdgcn_s_sleep(8);
                }
                if (lane == 0) st_agent(ws, tag);
            }
            return;
        }
        if (wv < 8) {
            BAR();
            IDS();
            auto rw = __builtin_amdgcn_make_buffer_rsrc((void*)qw, 0, K_ * N_ * 4, 0x00020000);
            const unsigned soA = kstep_off(wv + 4), soB = kstep_off(wv + 8);
            const unsigned voff = g * (4 * N_ * 4) + c * 16;
            Loader L;
            L.cs[0] = L.cs[1] = L.cs[2] = L.cs[3] = 0;
            auto ld1 = [&](int n) {
                L.wr[n >> 2][n & 3] = __builtin_amdgcn_raw_buffer_load_b128(rw, voff, (n < 16 ? soA : soB) + (32 * ((n >> 2) & 3) + (n & 3)) * (N_ * 4), 0);
                FENCE();
            };
            lds_wait_ge(ctl + C_OWN, 4);
#pragma unroll
            for (int n = 0; n < 16; ++n) ld1(n);
#pragma unroll
            for (int n = 0; n < 16; ++n) {
                VMWAIT(15);
                ld1(n + 16);
                if ((n & 3) == 3) L.packq(n >> 2);
            }
            L.stash(lds, wv + 4, lane);
            L.finish(lds, wv + 8, wv, lane);
            return;
        }
    }

    IDS();
    const int mw = wv - 8;
    u64 tag;
    if (mw == 0) {
        tag = ld_agent(ws);
        const unsigned par = *(const unsigned*)((lane < 32 ? swp : biasp) + n0 + 2 * (lane & 31));
        const unsigned psx = __builtin_nontemporal_load(sxp);
        const unsigned pzx = __builtin_nontemporal_load(zxp);
        FENCE();
        lds[LDS_CTL / 4 + lane] = 0;
        BAR();
        tag += TAG_STEP;
        lds[LDS_PAR / 4 + lane] = par;
        lds[LDS_PAR / 4 + 64] = psx;
        lds[LDS_PAR / 4 + 65] = pzx;
        FENCE();
        ctl[C_TAG_LO] = (unsigned)tag; ctl[C_TAG_HI] = (unsigned)(tag >> 32); ctl[C_PAR] = 1;
        if (lane == 0) st_agent(ws + WS_ACK + bid * 8, tag);
    } else {
        BAR();
        lds_wait_ge(ctl + C_PAR, 1);
        tag = ((u64)ctl[C_TAG_HI] << 32) | ctl[C_TAG_LO];
    }
    auto rx8 = __builtin_amdgcn_make_buffer_rsrc((void*)(ws + WS_X8), 0, M_ * K_, 0x00020000);
    const unsigned x8off = (mt * 8 + 2 * mw) * 16384 + lane * 16;
    v4i xa[16][2], bf[16][4], acc[2][4];
    auto ldx = [&](int u, int m) {
        xa[u][m] = __builtin_bit_cast(v4i, __builtin_amdgcn_raw_buffer_load_b128(rx8, x8off, ((4 * mt + u) & 15) * 1024 + m * 16384, SC1));
        FENCE();
    };
    auto ldb = [&](int u, int t) { bf[u][t] = *(const v4i*)(ldsb + u * 4096 + t * 1024 + lane * 16); FENCE(); };
#pragma unroll
    for (int t = 0; t < 8; ++t) acc[t >> 2][t & 3] = (v4i){0, 0, 0, 0};
    lds_wait_ge(ctl + C_OWN, 8);
    poll_flags(ws + WS_FLAG + (mt * 8 + 2 * mw + (lane & 1)) * FLAG_STRIDE, tag);
#pragma unroll
    for (int p = 0; p < 2 * XD; ++p) ldx(p >> 1, p & 1);
    v4f scv, ofv;
    unsigned rdy = 0;
    lds_wait_ge(ctl + C_GROUP, 4);
#pragma unroll
    for (int t = 0; t < 4; ++t) ldb(0, t);
#pragma unroll
    for (int u = 0; u < 16; ++u) {
#pragma unroll
        for (int k = 0; k < 8; ++k) {
            acc[k >> 2][k & 3] = __builtin_amdgcn_mfma_i32_16x16x64_i8(xa[u][k >> 2], bf[u][k & 3], acc[k >> 2][k & 3], 0, 0, 0);
            FENCE();
            if (k == 0 && u + 1 < 16 && ((u + 1) & 3) == 0) {
                if ((unsigned)__builtin_amdgcn_readfirstlane(rdy) < 4) lds_wait_ge(ctl + C_GROUP + ((u + 1) >> 2), 4);
                FENCE();
            }
            if (k < 4 && u + 1 < 16) ldb(u + 1, k);
            if ((k == 4 || k == 5) && u + XD < 16) ldx(u + XD, k - 4);
            if (k == 6 && u + 2 < 16 && ((u + 2) & 3) == 0) { rdy = ctl[C_GROUP + ((u + 2) >> 2)]; FENCE(); }
            if (k == 6 && u == 13) { rdy = ctl[C_EPI]; FENCE(); }
            if (k == 4 && u == 14) {
                if (__builtin_amdgcn_readfirstlane(rdy) == 0) lds_wait_ge(ctl + C_EPI, 1);
                FENCE();
                scv = *(const v4f*)(ldsb + LDS_EPI + 16 * c);
                ofv = *(const v4f*)(ldsb + LDS_EPI + 256 + 16 * c);
                FENCE();
            }
        }
    }
    auto ro = __builtin_amdgcn_make_buffer_rsrc((void*)out, 0, M_ * N_ * 2, 0x00020000);
    const unsigned ooff = ((mt * BM + 32 * mw + 4 * g) * N_ + n0 + 4 * c) * 2;
#pragma unroll
    for (int m = 0; m < 2; ++m)
#pragma unroll
        for (int r = 0; r < 4; ++r) {
            v2u o;
#pragma unroll
            for (int p = 0; p < 2; ++p) {
                const v2f v = (v2f){(float)acc[m][2 * p][r], (float)acc[m][2 * p + 1][r]} * (v2f){scv[2 * p], scv[2 * p + 1]}
                              + (v2f){ofv[2 * p], ofv[2 * p + 1]};
                o[p] = __builtin_bit_cast(unsigned, __builtin_convertvector(v, v2bf));
            }
            __builtin_amdgcn_raw_buffer_store_b64(o, ro, ooff, (16 * m + r) * N_ * 2, SC1);
        }
}

extern "C" void kernel_launch(void* const* d_in, const int* in_sizes, int n_in,
                              void* d_out, int out_size, void* d_ws, size_t ws_size,
                              hipStream_t stream) {
    w8a8_kernel<<<dim3(8, 4, 8), NT, 0, stream>>>((const int*)d_in[0], (const int*)d_in[1],
                                         (const unsigned short*)d_in[2], (const int*)d_in[3],
                                         (const unsigned short*)d_in[4], (const unsigned short*)d_in[5],
                                         (unsigned short*)d_out, (char*)d_ws);
}
```

```cpp
#include <hip/hip_runtime.h>
#include <hip/hip_bf16.h>
#include <stdint.h>

typedef int v4i __attribute__((ext_vector_type(4)));
typedef unsigned v4u __attribute__((ext_vector_type(4)));
typedef unsigned v2u __attribute__((ext_vector_type(2)));
typedef float v2f __attribute__((ext_vector_type(2)));
typedef float v4f __attribute__((ext_vector_type(4)));
typedef __bf16 v2bf __attribute__((ext_vector_type(2)));
typedef unsigned long long u64;
typedef __attribute__((address_space(1))) u64 gu64;
typedef __attribute__((address_space(3))) volatile unsigned lds_vu32;

constexpr int M_ = 512, K_ = 1024, N_ = 3584;
constexpr int BM = 128, BN = 64, NT = 12 * 64;
constexpr unsigned FLAG_STRIDE = 256, WS_FLAG = 4096, WS_ACK = 32768, WS_X8 = 262144;
constexpr u64 TAG_STEP = 0x9E3779B97F4A7C15ull;
constexpr int SC1 = 16;
constexpr unsigned SPIN_CAP = 1u << 15;
constexpr int LDS_CTL = 65536, LDS_PAR = LDS_CTL + 256, LDS_CS = LDS_PAR + 512, LDS_EPI = LDS_CS + 2048;
constexpr int LDS_BYTES = 84 * 1024;
enum { C_TAG_LO = 0, C_TAG_HI = 1, C_PAR = 2, C_OWN = 4, C_GROUP = 8, C_CSUM = 12, C_EPI = 13 };
constexpr int XD = 4;

#define PIN_ARGS() asm volatile("" :: "s"(qx), "s"(qw), "s"(sxp), "s"(zxp), "s"(swp), "s"(biasp), "s"(out), "s"(ws))
#define RT(x) asm volatile("s_memrealtime %0\n\ts_waitcnt lgkmcnt(0)" : "=s"(x) :: "memory")
#define SPINOUT() do { const u64 te_ = t0_ + 10000 + 10 * (tp_ - t0_); while (__builtin_amdgcn_s_memrealtime() < te_) __builtin_amdgcn_s_sleep(1); } while (0)
#define FENCE() do { asm volatile("" ::: "memory"); __builtin_amdgcn_sched_barrier(0); } while (0)
#define VMWAIT(n) do { asm volatile("s_waitcnt vmcnt(%0)" :: "i"(n) : "memory"); __builtin_amdgcn_sched_barrier(0); } while (0)
#define LGKM0() do { asm volatile("s_waitcnt lgkmcnt(0)" ::: "memory"); __builtin_amdgcn_sched_barrier(0); } while (0)
#define BAR() do { asm volatile("s_waitcnt lgkmcnt(0)\n\ts_barrier" ::: "memory"); __builtin_amdgcn_sched_barrier(0); } while (0)

__device__ __forceinline__ unsigned pack4(unsigned a, unsigned b, unsigned c, unsigned d) {
    return __builtin_amdgcn_perm(b, a, 0x0c0c0400u) | __builtin_amdgcn_perm(d, c, 0x04000c0cu);
}
__device__ __forceinline__ float rnd_bf16(float v) {
    const v2bf b = __builtin_convertvector((v2f){v, 0.f}, v2bf);
    return __builtin_bit_cast(float, __builtin_bit_cast(unsigned, b) << 16);
}
__device__ __forceinline__ float bf2f(unsigned short h) { return __builtin_bit_cast(float, (unsigned)h << 16); }
__device__ __forceinline__ u64 ld_agent(const void* p) { return __hip_atomic_load((gu64*)p, __ATOMIC_RELAXED, __HIP_MEMORY_SCOPE_AGENT); }
__device__ __forceinline__ void st_agent(void* p, u64 v) { __hip_atomic_store((gu64*)p, v, __ATOMIC_RELAXED, __HIP_MEMORY_SCOPE_AGENT); }
__device__ __forceinline__ void lds_wait_ge(lds_vu32* w, unsigned v) {
#pragma nounroll
    for (unsigned spin = 0; spin < (SPIN_CAP << 4); ++spin) {
        if ((unsigned)__builtin_amdgcn_readfirstlane(*w) >= v) break;
        __builtin_amdgcn_s_sleep(1);
    }
    FENCE();
}
__device__ __forceinline__ void poll_flags(const char* p, u64 tag) {
#pragma nounroll
    for (unsigned spin = 0; spin < SPIN_CAP; ++spin) {
        if (__all(ld_agent(p) == tag)) break;
        __builtin_amdgcn_s_sleep(8);
    }
    FENCE();
}

struct Loader {
    v4u wr[8][4], pk[4];
    int cs[4];
    __device__ __forceinline__ void packq(int i) {
#pragma unroll
        for (int t = 0; t < 4; ++t) {
            unsigned p = pack4(wr[i][0][t], wr[i][1][t], wr[i][2][t], wr[i][3][t]);
            cs[t] = __builtin_amdgcn_sdot4(p, 0x01010101, cs[t], false);
            asm volatile("" : "+v"(p), "+v"(cs[t]));
            pk[t][i & 3] = p;
        }
        FENCE();
    }
    __device__ __forceinline__ void stash(unsigned* lds, int slot, int lane) {
#pragma unroll
        for (int t = 0; t < 4; ++t) *(v4u*)((char*)lds + slot * 4096 + t * 1024 + lane * 16) = pk[t];
        LGKM0();
        if (lane == 0) atomicAdd(&lds[LDS_CTL / 4 + C_GROUP + (slot >> 2)], 1u);
        FENCE();
    }
    __device__ __forceinline__ void finish(unsigned* lds, int slot, int wv, int lane) {
        char* ldsb = (char*)lds;
        lds_vu32* ctl = (lds_vu32*)(lds + LDS_CTL / 4);
        packq(4); packq(5); packq(6); packq(7);
        stash(lds, slot, lane);
#pragma unroll
        for (int t = 0; t < 4; ++t) {
            auto r = __builtin_amdgcn_permlane32_swap(cs[t], cs[t], false, false);
            cs[t] = r[0] + r[1];
            r = __builtin_amdgcn_permlane16_swap(cs[t], cs[t], false, false);
            cs[t] = r[0] + r[1];
        }
        if (lane < 16) *(v4i*)(ldsb + LDS_CS + wv * 256 + lane * 16) = (v4i){cs[0], cs[1], cs[2], cs[3]};
        LGKM0();
        unsigned n = 0;
        if (lane == 0) n = atomicAdd(&lds[LDS_CTL / 4 + C_CSUM], 1u);
        if (__builtin_amdgcn_readfirstlane(n) == 7) {
            FENCE();
            int cst = 0;
#pragma unroll
            for (int k = 0; k < 8; ++k) cst += *(const int*)(ldsb + LDS_CS + k * 256 + lane * 4);
            lds_wait_ge(ctl + C_PAR, 1);
            const float sw = bf2f(*(const unsigned short*)(ldsb + LDS_PAR + 2 * lane));
            const float bi = bf2f(*(const unsigned short*)(ldsb + LDS_PAR + 128 + 2 * lane));
            const float sx = bf2f((unsigned short)lds[LDS_PAR / 4 + 64]);
            const float zc = rnd_bf16(rnd_bf16((float)cst) * rnd_bf16((float)(int)lds[LDS_PAR / 4 + 65]));
            const float sc = sw * sx;
            *(float*)(ldsb + LDS_EPI + lane * 4) = sc;
            *(float*)(ldsb + LDS_EPI + 256 + lane * 4) = bi - zc * sc;
            LGKM0();
            ctl[C_EPI] = 1;
        }
    }
};

__global__ __launch_bounds__(NT) __attribute__((aligned(4096)))
void w8a8_kernel(const int* __restrict__ qx, const int* __restrict__ qw,
                 const unsigned short* __restrict__ sxp, const int* __restrict__ zxp,
                 const unsigned short* __restrict__ swp, const unsigned short* __restrict__ biasp,
                 unsigned short* __restrict__ out, char* __restrict__ ws) {
    __shared__ __attribute__((aligned(16))) unsigned lds[LDS_BYTES / 4];
    u64 t0_ = __builtin_amdgcn_s_memrealtime(), tp_ = 0;
    const int wv = __builtin_amdgcn_readfirstlane(threadIdx.x >> 6);
    const bool conv = blockIdx.z == 0;
    char* ldsb = (char*)lds;
    lds_vu32* ctl = (lds_vu32*)(lds + LDS_CTL / 4);
#define IDS() \
    int tid_ = threadIdx.x, grp = blockIdx.x, mt = blockIdx.y, bz = blockIdx.z; \
    asm volatile("" : "+v"(tid_)); \
    asm volatile("" : "+s"(grp), "+s"(mt), "+s"(bz)); \
    const int lane = tid_ & 63, g = lane >> 4, c = lane & 15; \
    const int bid = (bz * 4 + mt) * 8 + grp, n0 = (grp * 7 + bz - 1) * BN; \
    auto kstep_off = [&](int slot) { const int s = (4 * mt + slot) & 15; return (unsigned)((128 * (s >> 1) + 16 * (s & 1)) * (N_ * 4) + n0 * 4); }; \
    (void)g; (void)c; (void)bid; (void)kstep_off

    {
        if (__builtin_expect(wv < (conv ? 8 : 4), 1)) {
            IDS();
            Loader L;
            unsigned voff, xh = 0;
            if (conv) {
                voff = (lane >> 3) * (K_ * 4) + (lane & 7) * 16;
                unsigned so = (mt * 8 + grp) * (16 * K_ * 4) + wv * 512;
                asm volatile("" : "+s"(so));
                asm volatile("" : "+v"(voff));
                PIN_ARGS();
                auto rx = __builtin_amdgcn_make_buffer_rsrc((void*)qx, 0, M_ * K_ * 4, 0x00020000);
#pragma unroll
                for (int n = 0; n < 8; ++n) {
                    L.wr[n >> 2][n & 3] = __builtin_amdgcn_raw_buffer_load_b128(rx, voff + (n & 3) * 128, so + (n >> 2) * (8 * K_ * 4), 0);
                    FENCE();
                }
            } else {
                voff = g * (4 * N_ * 4) + c * 16;
                unsigned so = (256 * mt + 128 * (wv >> 1) + 16 * (wv & 1)) * (N_ * 4) + n0 * 4;
                asm volatile("" : "+s"(so));
                asm volatile("" : "+v"(voff));
                PIN_ARGS();
                auto rw = __builtin_amdgcn_make_buffer_rsrc((void*)qw, 0, K_ * N_ * 4, 0x00020000);
#pragma unroll
                for (int n = 0; n < 8; ++n) {
                    L.wr[n >> 2][n & 3] = __builtin_amdgcn_raw_buffer_load_b128(rw, voff, so + (32 * (n >> 2) + (n & 3)) * (N_ * 4), 0);
                    FENCE();
                }
                if (wv == 0) {
                    auto rx = __builtin_amdgcn_make_buffer_rsrc((void*)qx, 0, M_ * K_ * 4, 0x00020000);
                    unsigned hl = (bz - 1) * 37 + (lane < 36 ? lane : 36);
                    hl = hl < 255u ? hl : 255u;
                    xh = __builtin_amdgcn_raw_buffer_load_b32(rx, ((hl & 0xf0) << 8) | ((hl & 15) << 7), (mt * 8 + grp) * (16 * K_ * 4) + 2048, 0);
                    FENCE();
                }
            }
            asm volatile("" ::: "memory");
            if (conv) {
                const int r8 = lane >> 3, q8 = lane & 7;
                auto rx8 = __builtin_amdgcn_make_buffer_rsrc((void*)(ws + WS_X8), 0, M_ * K_, 0x00020000);
                char* xt = ldsb + wv * 2048;
#pragma unroll
                for (int hh = 0; hh < 2; ++hh) {
                    v4u p;
#pragma unroll
                    for (int i = 0; i < 4; ++i) { const v4u v = L.wr[hh][i]; p[i] = pack4(v[0], v[1], v[2], v[3]); }
                    *(v4u*)(xt + q8 * 256 + (((8 * hh + r8) ^ q8) * 16)) = p;
                }
#pragma unroll
                for (int s = 0; s < 2; ++s) {
                    const int q = 4 * s + g;
                    const v4u a = *(const v4u*)(xt + q * 256 + ((c ^ q) * 16));
                    __builtin_amdgcn_raw_buffer_store_b128(a, rx8, ((bid * 16 + 2 * wv + s) * 64 + lane) * 16, 0, SC1);
                }
                VMWAIT(0);
                BAR();
                return;
            }
            BAR();
            auto rw = __builtin_amdgcn_make_buffer_rsrc((void*)qw, 0, K_ * N_ * 4, 0x00020000);
            const unsigned soA = kstep_off(wv);
#pragma unroll
            for (int n = 8; n < 16; ++n) {
                L.wr[n >> 2][n & 3] = __builtin_amdgcn_raw_buffer_load_b128(rw, voff, soA + (32 * (n >> 2) + (n & 3)) * (N_ * 4), 0);
                FENCE();
            }
            L.cs[0] = L.cs[1] = L.cs[2] = L.cs[3] = 0;
            L.packq(0);
            VMWAIT(8);
            if (lane == 0) atomicAdd(&lds[LDS_CTL / 4 + C_OWN], 1u);
            L.packq(1); L.packq(2); L.packq(3);
            if (wv == 1) RT(tp_);
            if (lane == 0) atomicAdd(&lds[LDS_CTL / 4 + C_OWN], 1u);
            L.stash(lds, wv, lane);
            asm volatile("" :: "v"(xh));
            const unsigned soB = kstep_off(wv + 4);
#pragma unroll
            for (int n = 0; n < 16; ++n) {
                L.wr[4 + (n >> 2)][n & 3] = __builtin_amdgcn_raw_buffer_load_b128(rw, voff, soB + (32 * (n >> 2) + (n & 3)) * (N_ * 4), 0);
                FENCE();
            }
            L.finish(lds, wv + 4, wv, lane);
            if (wv == 1) SPINOUT();
            return;
        }
        PIN_ARGS();
        if (conv) {
            if (wv != 8) return;
            IDS();
            const u64 tag = ld_agent(ws) + TAG_STEP;
            BAR();
            if (lane == 0) { st_agent(ws + WS_FLAG + bid * FLAG_STRIDE, tag); st_agent(ws + WS_ACK + bid * 8, tag); }
            if (bid == 0) {
                __builtin_amdgcn_s_sleep(60);
#pragma nounroll
                for (unsigned spin = 0; spin < SPIN_CAP; ++spin) {
                    bool ok = true;
#pragma unroll
                    for (int k = 0; k < 4; ++k) ok &= (ld_agent(ws + WS_ACK + (k * 64 + lane) * 8) == tag);
                    if (__all(ok)) break;
                    __builtin_amdgcn_s_sleep(8);
                }
                if (lane == 0) st_agent(ws, tag);
            }
            return;
        }
        if (wv < 8) {
            BAR();
            IDS();
            auto rw = __builtin_amdgcn_make_buffer_rsrc((void*)qw, 0, K_ * N_ * 4, 0x00020000);
            const unsigned soA = kstep_off(wv + 4), soB = kstep_off(wv + 8);
            const unsigned voff = g * (4 * N_ * 4) + c * 16;
            Loader L;
            L.cs[0] = L.cs[1] = L.cs[2] = L.cs[3] = 0;
            auto ld1 = [&](int n) {
                L.wr[n >> 2][n & 3] = __builtin_amdgcn_raw_buffer_load_b128(rw, voff, (n < 16 ? soA : soB) + (32 * ((n >> 2) & 3) + (n & 3)) * (N_ * 4), 0);
                FENCE();
            };
            lds_wait_ge(ctl + C_OWN, 4);
#pragma unroll
            for (int n = 0; n < 16; ++n) ld1(n);
#pragma unroll
            for (int n = 0; n < 16; ++n) {
                VMWAIT(15);
                ld1(n + 16);
                if ((n & 3) == 3) L.packq(n >> 2);
            }
            L.stash(lds, wv + 4, lane);
            L.finish(lds, wv + 8, wv, lane);
            return;
        }
    }

    IDS();
    const int mw = wv - 8;
    u64 tag;
    if (mw == 0) {
        tag = ld_agent(ws);
        const unsigned par = *(const unsigned*)((lane < 32 ? swp : biasp) + n0 + 2 * (lane & 31));
        const unsigned psx = __builtin_nontemporal_load(sxp);
        const unsigned pzx = __builtin_nontemporal_load(zxp);
        FENCE();
        lds[LDS_CTL / 4 + lane] = 0;
        BAR();
        tag += TAG_STEP;
        lds[LDS_PAR / 4 + lane] = par;
        lds[LDS_PAR / 4 + 64] = psx;
        lds[LDS_PAR / 4 + 65] = pzx;
        FENCE();
        ctl[C_TAG_LO] = (unsigned)tag; ctl[C_TAG_HI] = (unsigned)(tag >> 32); ctl[C_PAR] = 1;
        if (lane == 0) st_agent(ws + WS_ACK + bid * 8, tag);
    } else {
        BAR();
        lds_wait_ge(ctl + C_PAR, 1);
        tag = ((u64)ctl[C_TAG_HI] << 32) | ctl[C_TAG_LO];
    }
    auto rx8 = __builtin_amdgcn_make_buffer_rsrc((void*)(ws + WS_X8), 0, M_ * K_, 0x00020000);
    const unsigned x8off = (mt * 8 + 2 * mw) * 16384 + lane * 16;
    v4i xa[16][2], bf[16][4], acc[2][4];
    auto ldx = [&](int u, int m) {
        xa[u][m] = __builtin_bit_cast(v4i, __builtin_amdgcn_raw_buffer_load_b128(rx8, x8off, ((4 * mt + u) & 15) * 1024 + m * 16384, SC1));
        FENCE();
    };
    auto ldb = [&](int u, int t) { bf[u][t] = *(const v4i*)(ldsb + u * 4096 + t * 1024 + lane * 16); FENCE(); };
#pragma unroll
    for (int t = 0; t < 8; ++t) acc[t >> 2][t & 3] = (v4i){0, 0, 0, 0};
    lds_wait_ge(ctl + C_OWN, 8);
    poll_flags(ws + WS_FLAG + (mt * 8 + 2 * mw + (lane & 1)) * FLAG_STRIDE, tag);
#pragma unroll
    for (int p = 0; p < 2 * XD; ++p) ldx(p >> 1, p & 1);
    v4f scv, ofv;
    unsigned rdy = 0;
    lds_wait_ge(ctl + C_GROUP, 4);
#pragma unroll
    for (int t = 0; t < 4; ++t) ldb(0, t);
#pragma unroll
    for (int u = 0; u < 16; ++u) {
#pragma unroll
        for (int k = 0; k < 8; ++k) {
            acc[k >> 2][k & 3] = __builtin_amdgcn_mfma_i32_16x16x64_i8(xa[u][k >> 2], bf[u][k & 3], acc[k >> 2][k & 3], 0, 0, 0);
            FENCE();
            if (k == 0 && u + 1 < 16 && ((u + 1) & 3) == 0) {
                if ((unsigned)__builtin_amdgcn_readfirstlane(rdy) < 4) lds_wait_ge(ctl + C_GROUP + ((u + 1) >> 2), 4);
                FENCE();
            }
            if (k < 4 && u + 1 < 16) ldb(u + 1, k);
            if ((k == 4 || k == 5) && u + XD < 16) ldx(u + XD, k - 4);
            if (k == 6 && u + 2 < 16 && ((u + 2) & 3) == 0) { rdy = ctl[C_GROUP + ((u + 2) >> 2)]; FENCE(); }
            if (k == 6 && u == 13) { rdy = ctl[C_EPI]; FENCE(); }
            if (k == 4 && u == 14) {
                if (__builtin_amdgcn_readfirstlane(rdy) == 0) lds_wait_ge(ctl + C_EPI, 1);
                FENCE();
                scv = *(const v4f*)(ldsb + LDS_EPI + 16 * c);
                ofv = *(const v4f*)(ldsb + LDS_EPI + 256 + 16 * c);
                FENCE();
            }
        }
    }
    auto ro = __builtin_amdgcn_make_buffer_rsrc((void*)out, 0, M_ * N_ * 2, 0x00020000);
    const unsigned ooff = ((mt * BM + 32 * mw + 4 * g) * N_ + n0 + 4 * c) * 2;
#pragma unroll
    for (int m = 0; m < 2; ++m)
#pragma unroll
        for (int r = 0; r < 4; ++r) {
            v2u o;
#pragma unroll
            for (int p = 0; p < 2; ++p) {
                const v2f v = (v2f){(float)acc[m][2 * p][r], (float)acc[m][2 * p + 1][r]} * (v2f){scv[2 * p], scv[2 * p + 1]}
                              + (v2f){ofv[2 * p], ofv[2 * p + 1]};
                o[p] = __builtin_bit_cast(unsigned, __builtin_convertvector(v, v2bf));
            }
            __builtin_amdgcn_raw_buffer_store_b64(o, ro, ooff, (16 * m + r) * N_ * 2, SC1);
        }
}

extern "C" void kernel_launch(void* const* d_in, const int* in_sizes, int n_in,
                              void* d_out, int out_size, void* d_ws, size_t ws_size,
                              hipStream_t stream) {
    w8a8_kernel<<<dim3(8, 4, 8), NT, 0, stream>>>((const int*)d_in[0], (const int*)d_in[1],
                                         (const unsigned short*)d_in[2], (const int*)d_in[3],
                                         (const unsigned short*)d_in[4], (const unsigned short*)d_in[5],
                                         (unsigned short*)d_out, (char*)d_ws);
}
```

```cpp
#include <hip/hip_runtime.h>
#include <hip/hip_bf16.h>
#include <stdint.h>

typedef int v4i __attribute__((ext_vector_type(4)));
typedef unsigned v4u __attribute__((ext_vector_type(4)));
typedef unsigned v2u __attribute__((ext_vector_type(2)));
typedef float v2f __attribute__((ext_vector_type(2)));
typedef float v4f __attribute__((ext_vector_type(4)));
typedef __bf16 v2bf __attribute__((ext_vector_type(2)));
typedef unsigned long long u64;
typedef __attribute__((address_space(1))) u64 gu64;
typedef __attribute__((address_space(3))) volatile unsigned lds_vu32;

constexpr int M_ = 512, K_ = 1024, N_ = 3584;
constexpr int BM = 128, BN = 64, NT = 12 * 64;
constexpr unsigned FLAG_STRIDE = 256, WS_FLAG = 4096, WS_ACK = 32768, WS_X8 = 262144;
constexpr u64 TAG_STEP = 0x9E3779B97F4A7C15ull;
constexpr int SC1 = 16;
constexpr unsigned SPIN_CAP = 1u << 15;
constexpr int LDS_CTL = 65536, LDS_CS = LDS_CTL + 256, LDS_PAR = LDS_CS + 256;
constexpr int LDS_BYTES = 84 * 1024;
enum { C_TAG_LO = 0, C_TAG_HI = 1, C_PAR = 2, C_OWN = 4, C_GROUP = 8, C_CSUM = 12 };
constexpr int XD = 4;

#define PIN_ARGS() asm volatile("" :: "s"(qx), "s"(qw), "s"(sxp), "s"(zxp), "s"(swp), "s"(biasp), "s"(out), "s"(ws))
#define FENCE() do { asm volatile("" ::: "memory"); __builtin_amdgcn_sched_barrier(0); } while (0)
#define VMWAIT(n) do { asm volatile("s_waitcnt vmcnt(%0)" :: "i"(n) : "memory"); __builtin_amdgcn_sched_barrier(0); } while (0)
#define LGKM0() do { asm volatile("s_waitcnt lgkmcnt(0)" ::: "memory"); __builtin_amdgcn_sched_barrier(0); } while (0)
#define BAR() do { asm volatile("s_waitcnt lgkmcnt(0)\n\ts_barrier" ::: "memory"); __builtin_amdgcn_sched_barrier(0); } while (0)

__device__ __forceinline__ unsigned pack4(unsigned a, unsigned b, unsigned c, unsigned d) {
    return __builtin_amdgcn_perm(b, a, 0x0c0c0400u) | __builtin_amdgcn_perm(d, c, 0x04000c0cu);
}
__device__ __forceinline__ float rnd_bf16(float v) {
    const v2bf b = __builtin_convertvector((v2f){v, 0.f}, v2bf);
    return __builtin_bit_cast(float, __builtin_bit_cast(unsigned, b) << 16);
}
__device__ __forceinline__ float bf2f(unsigned short h) { return __builtin_bit_cast(float, (unsigned)h << 16); }
__device__ __forceinline__ u64 ld_agent(const void* p) { return __hip_atomic_load((gu64*)p, __ATOMIC_RELAXED, __HIP_MEMORY_SCOPE_AGENT); }
__device__ __forceinline__ void st_agent(void* p, u64 v) { __hip_atomic_store((gu64*)p, v, __ATOMIC_RELAXED, __HIP_MEMORY_SCOPE_AGENT); }
__device__ __forceinline__ void lds_wait_ge(lds_vu32* w, unsigned v) {
#pragma nounroll
    for (unsigned spin = 0; spin < (SPIN_CAP << 4); ++spin) {
        if ((unsigned)__builtin_amdgcn_readfirstlane(*w) >= v) break;
        __builtin_amdgcn_s_sleep(1);
    }
    FENCE();
}
__device__ __forceinline__ void poll_flags(const char* p, u64 tag) {
#pragma nounroll
    for (unsigned spin = 0; spin < SPIN_CAP; ++spin) {
        if (__all(ld_agent(p) == tag)) break;
        __builtin_amdgcn_s_sleep(8);
    }
    FENCE();
}

struct Loader {
    v4u wr[8][4], pk[4];
    int cs[4];
    __device__ __forceinline__ void packq(int i) {
#pragma unroll
        for (int t = 0; t < 4; ++t) {
            unsigned p = pack4(wr[i][0][t], wr[i][1][t], wr[i][2][t], wr[i][3][t]);
            cs[t] = __builtin_amdgcn_sdot4(p, 0x01010101, cs[t], false);
            asm volatile("" : "+v"(p), "+v"(cs[t]));
            pk[t][i & 3] = p;
        }
        FENCE();
    }
    __device__ __forceinline__ void stash(unsigned* lds, int slot, int lane) {
#pragma unroll
        for (int t = 0; t < 4; ++t) *(v4u*)((char*)lds + slot * 4096 + t * 1024 + lane * 16) = pk[t];
        LGKM0();
        if (lane == 0) atomicAdd(&lds[LDS_CTL / 4 + C_GROUP + (slot >> 2)], 1u);
        FENCE();
    }
    __device__ __forceinline__ void finish(unsigned* lds, int slot, int lane) {
        packq(4); packq(5); packq(6); packq(7);
        stash(lds, slot, lane);
#pragma unroll
        for (int t = 0; t < 4; ++t) {
            auto r = __builtin_amdgcn_permlane32_swap(cs[t], cs[t], false, false);
            cs[t] = r[0] + r[1];
            r = __builtin_amdgcn_permlane16_swap(cs[t], cs[t], false, false);
            cs[t] = r[0] + r[1];
        }
        const int lo = (lane & 16) ? cs[1] : cs[0], hi = (lane & 16) ? cs[3] : cs[2];
        atomicAdd(&lds[LDS_CS / 4 + 4 * (lane & 15) + (lane >> 4)], (unsigned)((lane & 32) ? hi : lo));
        FENCE();
        if (lane == 0) atomicAdd(&lds[LDS_CTL / 4 + C_CSUM], 1u);
    }
};

__global__ __launch_bounds__(NT) __attribute__((aligned(4096)))
void w8a8_kernel(const int* __restrict__ qx, const int* __restrict__ qw,
                 const unsigned short* __restrict__ sxp, const int* __restrict__ zxp,
                 const unsigned short* __restrict__ swp, const unsigned short* __restrict__ biasp,
                 unsigned short* __restrict__ out, char* __restrict__ ws) {
    __shared__ __attribute__((aligned(16))) unsigned lds[LDS_BYTES / 4];
    const int wv = __builtin_amdgcn_readfirstlane(threadIdx.x >> 6);
    const bool conv = blockIdx.z == 0;
    char* ldsb = (char*)lds;
    lds_vu32* ctl = (lds_vu32*)(lds + LDS_CTL / 4);
#define IDS() \
    int tid_ = threadIdx.x, grp = blockIdx.x, mt = blockIdx.y, bz = blockIdx.z; \
    asm volatile("" : "+v"(tid_)); \
    asm volatile("" : "+s"(grp), "+s"(mt), "+s"(bz)); \
    const int lane = tid_ & 63, g = lane >> 4, c = lane & 15; \
    const int bid = (bz * 4 + mt) * 8 + grp, n0 = (grp * 7 + bz - 1) * BN; \
    auto kstep_off = [&](int slot) { const int s = (4 * mt + slot) & 15; return (unsigned)((128 * (s >> 1) + 16 * (s & 1)) * (N_ * 4) + n0 * 4); }; \
    (void)g; (void)c; (void)bid; (void)kstep_off

    {
        if (__builtin_expect(wv < (conv ? 8 : 4), 1)) {
            IDS();
            Loader L;
            unsigned voff, xh = 0;
            if (conv) {
                voff = (lane >> 3) * (K_ * 4) + (lane & 7) * 16;
                unsigned so = (mt * 8 + grp) * (16 * K_ * 4) + wv * 512;
                asm volatile("" : "+s"(so));
                asm volatile("" : "+v"(voff));
                PIN_ARGS();
                auto rx = __builtin_amdgcn_make_buffer_rsrc((void*)qx, 0, M_ * K_ * 4, 0x00020000);
#pragma unroll
                for (int n = 0; n < 8; ++n) {
                    L.wr[n >> 2][n & 3] = __builtin_amdgcn_raw_buffer_load_b128(rx, voff + (n & 3) * 128, so + (n >> 2) * (8 * K_ * 4), 0);
                    FENCE();
                }
            } else {
                voff = g * (4 * N_ * 4) + c * 16;
                unsigned so = (256 * mt + 128 * (wv >> 1) + 16 * (wv & 1)) * (N_ * 4) + n0 * 4;
                asm volatile("" : "+s"(so));
                asm volatile("" : "+v"(voff));
                PIN_ARGS();
                auto rw = __builtin_amdgcn_make_buffer_rsrc((void*)qw, 0, K_ * N_ * 4, 0x00020000);
#pragma unroll
                for (int n = 0; n < 8; ++n) {
                    L.wr[n >> 2][n & 3] = __builtin_amdgcn_raw_buffer_load_b128(rw, voff, so + (32 * (n >> 2) + (n & 3)) * (N_ * 4), 0);
                    FENCE();
                }
                if (wv == 0) {
                    auto rx = __builtin_amdgcn_make_buffer_rsrc((void*)qx, 0, M_ * K_ * 4, 0x00020000);
                    unsigned hl = (bz - 1) * 37 + (lane < 36 ? lane : 36);
                    hl = hl < 255u ? hl : 255u;
                    xh = __builtin_amdgcn_raw_buffer_load_b32(rx, ((hl & 0xf0) << 8) | ((hl & 15) << 7), (mt * 8 + grp) * (16 * K_ * 4) + 2048, 0);
                    FENCE();
                }
            }
            asm volatile("" ::: "memory");
            if (conv) {
                const int r8 = lane >> 3, q8 = lane & 7;
                auto rx8 = __builtin_amdgcn_make_buffer_rsrc((void*)(ws + WS_X8), 0, M_ * K_, 0x00020000);
                char* xt = ldsb + wv * 2048;
#pragma unroll
                for (int hh = 0; hh < 2; ++hh) {
                    v4u p;
#pragma unroll
                    for (int i = 0; i < 4; ++i) { const v4u v = L.wr[hh][i]; p[i] = pack4(v[0], v[1], v[2], v[3]); }
                    *(v4u*)(xt + q8 * 256 + (((8 * hh + r8) ^ q8) * 16)) = p;
                }
#pragma unroll
                for (int s = 0; s < 2; ++s) {
                    const int q = 4 * s + g;
                    const v4u a = *(const v4u*)(xt + q * 256 + ((c ^ q) * 16));
                    __builtin_amdgcn_raw_buffer_store_b128(a, rx8, ((bid * 16 + 2 * wv + s) * 64 + lane) * 16, 0, SC1);
                }
                VMWAIT(0);
                BAR();
                return;
            }
            BAR();
            auto rw = __builtin_amdgcn_make_buffer_rsrc((void*)qw, 0, K_ * N_ * 4, 0x00020000);
            const unsigned soA = kstep_off(wv);
#pragma unroll
            for (int n = 8; n < 16; ++n) {
                L.wr[n >> 2][n & 3] = __builtin_amdgcn_raw_buffer_load_b128(rw, voff, soA + (32 * (n >> 2) + (n & 3)) * (N_ * 4), 0);
                FENCE();
            }
            L.cs[0] = L.cs[1] = L.cs[2] = L.cs[3] = 0;
            L.packq(0);
            VMWAIT(8);
            if (lane == 0) atomicAdd(&lds[LDS_CTL / 4 + C_OWN], 1u);
            L.packq(1); L.packq(2); L.packq(3);
            if (lane == 0) atomicAdd(&lds[LDS_CTL / 4 + C_OWN], 1u);
            L.stash(lds, wv, lane);
            asm volatile("" :: "v"(xh));
            const unsigned soB = kstep_off(wv + 4);
#pragma unroll
            for (int n = 0; n < 16; ++n) {
                L.wr[4 + (n >> 2)][n & 3] = __builtin_amdgcn_raw_buffer_load_b128(rw, voff, soB + (32 * (n >> 2) + (n & 3)) * (N_ * 4), 0);
                FENCE();
            }
            L.finish(lds, wv + 4, lane);
            return;
        }
        PIN_ARGS();
        if (conv) {
            if (wv != 8) return;
            IDS();
            const u64 tag = ld_agent(ws) + TAG_STEP;
            BAR();
            if (lane == 0) { st_agent(ws + WS_FLAG + bid * FLAG_STRIDE, tag); st_agent(ws + WS_ACK + bid * 8, tag); }
            if (bid == 0) {
                __builtin_amdgcn_s_sleep(60);
#pragma nounroll
                for (unsigned spin = 0; spin < SPIN_CAP; ++spin) {
                    bool ok = true;
#pragma unroll
                    for (int k = 0; k < 4; ++k) ok &= (ld_agent(ws + WS_ACK + (k * 64 + lane) * 8) == tag);
                    if (__all(ok)) break;
                    __builtin_amdgcn_s_sleep(8);
                }
                if (lane == 0) st_agent(ws, tag);
            }
            return;
        }
        if (wv < 8) {
            BAR();
            IDS();
            auto rw = __builtin_amdgcn_make_buffer_rsrc((void*)qw, 0, K_ * N_ * 4, 0x00020000);
            const unsigned soA = kstep_off(wv + 4), soB = kstep_off(wv + 8);
            const unsigned voff = g * (4 * N_ * 4) + c * 16;
            Loader L;
            L.cs[0] = L.cs[1] = L.cs[2] = L.cs[3] = 0;
            auto ld1 = [&](int n) {
                L.wr[n >> 2][n & 3] = __builtin_amdgcn_raw_buffer_load_b128(rw, voff, (n < 16 ? soA : soB) + (32 * ((n >> 2) & 3) + (n & 3)) * (N_ * 4), 0);
                FENCE();
            };
            lds_wait_ge(ctl + C_OWN, 4);
#pragma unroll
            for (int n = 0; n < 16; ++n) ld1(n);
#pragma unroll
            for (int n = 0; n < 16; ++n) {
                VMWAIT(15);
                ld1(n + 16);
                if ((n & 3) == 3) L.packq(n >> 2);
            }
            L.stash(lds, wv + 4, lane);
            L.finish(lds, wv + 8, lane);
            return;
        }
    }

    IDS();
    const int mw = wv - 8;
    u64 tag;
    if (mw == 0) {
        tag = ld_agent(ws);
        const unsigned par = *(const unsigned*)((lane < 32 ? swp : biasp) + n0 + 2 * (lane & 31));
        const unsigned psx = __builtin_nontemporal_load(sxp);
        const unsigned pzx = __builtin_nontemporal_load(zxp);
        FENCE();
        lds[LDS_CTL / 4 + lane] = 0;
        lds[LDS_CS / 4 + lane] = 0;
        BAR();
        tag += TAG_STEP;
        lds[LDS_PAR / 4 + lane] = par;
        lds[LDS_PAR / 4 + 64] = psx;
        lds[LDS_PAR / 4 + 65] = pzx;
        FENCE();
        ctl[C_TAG_LO] = (unsigned)tag; ctl[C_TAG_HI] = (unsigned)(tag >> 32); ctl[C_PAR] = 1;
        if (lane == 0) st_agent(ws + WS_ACK + bid * 8, tag);
    } else {
        BAR();
        lds_wait_ge(ctl + C_PAR, 1);
        tag = ((u64)ctl[C_TAG_HI] << 32) | ctl[C_TAG_LO];
    }
    auto rx8 = __builtin_amdgcn_make_buffer_rsrc((void*)(ws + WS_X8), 0, M_ * K_, 0x00020000);
    const unsigned x8off = (mt * 8 + 2 * mw) * 16384 + lane * 16;
    v4i xa[16][2], bf[16][4], acc[2][4];
    auto ldx = [&](int u, int m) {
        xa[u][m] = __builtin_bit_cast(v4i, __builtin_amdgcn_raw_buffer_load_b128(rx8, x8off, ((4 * mt + u) & 15) * 1024 + m * 16384, SC1));
        FENCE();
    };
    auto ldb = [&](int u, int t) { bf[u][t] = *(const v4i*)(ldsb + u * 4096 + t * 1024 + lane * 16); FENCE(); };
#pragma unroll
    for (int t = 0; t < 8; ++t) acc[t >> 2][t & 3] = (v4i){0, 0, 0, 0};
    v4f scv, ofv;
    float zf;
    {
        const v2u swv = *(const v2u*)(ldsb + LDS_PAR + 8 * c), biv = *(const v2u*)(ldsb + LDS_PAR + 128 + 8 * c);
        const v2u szv = *(const v2u*)(ldsb + LDS_PAR + 256);
        const float sx = bf2f((unsigned short)szv[0]);
        zf = rnd_bf16((float)(int)szv[1]);
#pragma unroll
        for (int t = 0; t < 4; ++t) {
            scv[t] = __builtin_bit_cast(float, (t & 1) ? (swv[t >> 1] & 0xffff0000u) : (swv[t >> 1] << 16)) * sx;
            ofv[t] = __builtin_bit_cast(float, (t & 1) ? (biv[t >> 1] & 0xffff0000u) : (biv[t >> 1] << 16));
        }
        asm volatile("" : "+v"(scv), "+v"(ofv), "+v"(zf));
    }
    lds_wait_ge(ctl + C_OWN, 8);
    poll_flags(ws + WS_FLAG + (mt * 8 + 2 * mw + (lane & 1)) * FLAG_STRIDE, tag);
#pragma unroll
    for (int p = 0; p < 2 * XD; ++p) ldx(p >> 1, p & 1);
    v4i csv;
    v2f ev[2] = {{0.f, 0.f}, {0.f, 0.f}};
    unsigned eu[2] = {0u, 0u};
    unsigned rdy = 0;
    lds_wait_ge(ctl + C_GROUP, 4);
#pragma unroll
    for (int t = 0; t < 4; ++t) ldb(0, t);
#pragma unroll
    for (int u = 0; u < 16; ++u) {
#pragma unroll
        for (int k = 0; k < 8; ++k) {
            acc[k >> 2][k & 3] = __builtin_amdgcn_mfma_i32_16x16x64_i8(xa[u][k >> 2], bf[u][k & 3], acc[k >> 2][k & 3], 0, 0, 0);
            FENCE();
            if (k == 0 && u + 1 < 16 && ((u + 1) & 3) == 0) {
                if ((unsigned)__builtin_amdgcn_readfirstlane(rdy) < 4) lds_wait_ge(ctl + C_GROUP + ((u + 1) >> 2), 4);
                FENCE();
            }
            if (k < 4 && u + 1 < 16) ldb(u + 1, k);
            if ((k == 4 || k == 5) && u + XD < 16) ldx(u + XD, k - 4);
            if (k == 6 && u + 2 < 16 && ((u + 2) & 3) == 0) { rdy = ctl[C_GROUP + ((u + 2) >> 2)]; FENCE(); }
            if (k == 6 && u == 13) { rdy = ctl[C_CSUM]; FENCE(); }
            if (k == 3 && u == 14) {
                if ((unsigned)__builtin_amdgcn_readfirstlane(rdy) < 8) lds_wait_ge(ctl + C_CSUM, 8);
                FENCE();
                csv = *(const v4i*)(ldsb + LDS_CS + 16 * c);
                FENCE();
            }
            if (u * 8 + k >= 14 * 8 + 7 && u * 8 + k < 14 * 8 + 7 + 7) {
                const int ph = u * 8 + k - (14 * 8 + 7);
#pragma unroll
                for (int p = 0; p < 2; ++p) {
                    if (ph == 0) ev[p].x = (float)csv[2 * p];
                    if (ph == 1) ev[p].y = (float)csv[2 * p + 1];
                    if (ph == 2 || ph == 5) eu[p] = __builtin_bit_cast(unsigned, __builtin_convertvector(ph == 2 ? ev[p] : ev[p] * zf, v2bf));
                    if (ph == 3 || ph == 6) ev[p].x = __builtin_bit_cast(float, eu[p] << 16);
                    if (ph == 4 || ph == 6) ev[p].y = __builtin_bit_cast(float, eu[p] & 0xffff0000u);
                    asm volatile("" : "+v"(ev[p]), "+v"(eu[p]));
                }
                FENCE();
            }
        }
    }
#pragma unroll
    for (int t = 0; t < 4; ++t) ofv[t] -= ev[t >> 1][t & 1] * scv[t];
    auto ro = __builtin_amdgcn_make_buffer_rsrc((void*)out, 0, M_ * N_ * 2, 0x00020000);
    const unsigned ooff = ((mt * BM + 32 * mw + 4 * g) * N_ + n0 + 4 * c) * 2;
#pragma unroll
    for (int m = 0; m < 2; ++m)
#pragma unroll
        for (int r = 0; r < 4; ++r) {
            v2u o;
#pragma unroll
            for (int p = 0; p < 2; ++p) {
                const v2f v = (v2f){(float)acc[m][2 * p][r], (float)acc[m][2 * p + 1][r]} * (v2f){scv[2 * p], scv[2 * p + 1]}
                              + (v2f){ofv[2 * p], ofv[2 * p + 1]};
                o[p] = __builtin_bit_cast(unsigned, __builtin_convertvector(v, v2bf));
            }
            __builtin_amdgcn_raw_buffer_store_b64(o, ro, ooff, (16 * m + r) * N_ * 2, SC1);
        }
}

extern "C" void kernel_launch(void* const* d_in, const int* in_sizes, int n_in,
                              void* d_out, int out_size, void* d_ws, size_t ws_size,
                              hipStream_t stream) {
    w8a8_kernel<<<dim3(8, 4, 8), NT, 0, stream>>>((const int*)d_in[0], (const int*)d_in[1],
                                         (const unsigned short*)d_in[2], (const int*)d_in[3],
                                         (const unsigned short*)d_in[4], (const unsigned short*)d_in[5],
                                         (unsigned short*)d_out, (char*)d_ws);
}
```

```cpp
#include <hip/hip_runtime.h>
#include <hip/hip_bf16.h>
#include <stdint.h>

typedef int v4i __attribute__((ext_vector_type(4)));
typedef unsigned v4u __attribute__((ext_vector_type(4)));
typedef unsigned v2u __attribute__((ext_vector_type(2)));
typedef float v2f __attribute__((ext_vector_type(2)));
typedef float v4f __attribute__((ext_vector_type(4)));
typedef __bf16 v2bf __attribute__((ext_vector_type(2)));
typedef unsigned long long u64;
typedef __attribute__((address_space(1))) u64 gu64;
typedef __attribute__((address_space(3))) volatile unsigned lds_vu32;

constexpr int M_ = 512, K_ = 1024, N_ = 3584;
constexpr int BM = 128, BN = 64, NT = 12 * 64;
constexpr unsigned FLAG_STRIDE = 256, WS_FLAG = 4096, WS_ACK = 32768, WS_X8 = 262144;
constexpr u64 TAG_STEP = 0x9E3779B97F4A7C15ull;
constexpr int SC1 = 16;
constexpr unsigned SPIN_CAP = 1u << 15;
constexpr int LDS_CTL = 65536, LDS_CS = LDS_CTL + 256, LDS_PAR = LDS_CS + 256;
constexpr int LDS_BYTES = 84 * 1024;
enum { C_TAG_LO = 0, C_TAG_HI = 1, C_PAR = 2, C_OWN = 4, C_GROUP = 8, C_CSUM = 12 };
constexpr int XD = 4;

#define PIN_ARGS() asm volatile("" :: "s"(qx), "s"(qw), "s"(sxp), "s"(zxp), "s"(swp), "s"(biasp), "s"(out), "s"(ws))
#define FENCE() do { asm volatile("" ::: "memory"); __builtin_amdgcn_sched_barrier(0); } while (0)
#define VMWAIT(n) do { asm volatile("s_waitcnt vmcnt(%0)" :: "i"(n) : "memory"); __builtin_amdgcn_sched_barrier(0); } while (0)
#define LGKM0() do { asm volatile("s_waitcnt lgkmcnt(0)" ::: "memory"); __builtin_amdgcn_sched_barrier(0); } while (0)
#define BAR() do { asm volatile("s_waitcnt lgkmcnt(0)\n\ts_barrier" ::: "memory"); __builtin_amdgcn_sched_barrier(0); } while (0)

__device__ __forceinline__ unsigned pack4(unsigned a, unsigned b, unsigned c, unsigned d) {
    return __builtin_amdgcn_perm(b, a, 0x0c0c0400u) | __builtin_amdgcn_perm(d, c, 0x04000c0cu);
}
__device__ __forceinline__ float rnd_bf16(float v) {
    const v2bf b = __builtin_convertvector((v2f){v, 0.f}, v2bf);
    return __builtin_bit_cast(float, __builtin_bit_cast(unsigned, b) << 16);
}
__device__ __forceinline__ float bf2f(unsigned short h) { return __builtin_bit_cast(float, (unsigned)h << 16); }
__device__ __forceinline__ u64 ld_agent(const void* p) { return __hip_atomic_load((gu64*)p, __ATOMIC_RELAXED, __HIP_MEMORY_SCOPE_AGENT); }
__device__ __forceinline__ void st_agent(void* p, u64 v) { __hip_atomic_store((gu64*)p, v, __ATOMIC_RELAXED, __HIP_MEMORY_SCOPE_AGENT); }
__device__ __forceinline__ void lds_wait_ge(lds_vu32* w, unsigned v) {
#pragma nounroll
    for (unsigned spin = 0; spin < (SPIN_CAP << 4); ++spin) {
        if ((unsigned)__builtin_amdgcn_readfirstlane(*w) >= v) break;
        __builtin_amdgcn_s_sleep(1);
    }
    FENCE();
}
__device__ __forceinline__ void poll_flags(const char* p, u64 tag) {
#pragma nounroll
    for (unsigned spin = 0; spin < SPIN_CAP; ++spin) {
        if (__all(ld_agent(p) == tag)) break;
        __builtin_amdgcn_s_sleep(8);
    }
    FENCE();
}

struct Loader {
    v4u wr[8][4], pk[4];
    int cs[4];
    __device__ __forceinline__ void packq(int i) {
#pragma unroll
        for (int t = 0; t < 4; ++t) {
            unsigned p = pack4(wr[i][0][t], wr[i][1][t], wr[i][2][t], wr[i][3][t]);
            cs[t] = __builtin_amdgcn_sdot4(p, 0x01010101, cs[t], false);
            asm volatile("" : "+v"(p), "+v"(cs[t]));
            pk[t][i & 3] = p;
        }
        FENCE();
    }
    __device__ __forceinline__ void stash(unsigned* lds, int slot, int lane) {
#pragma unroll
        for (int t = 0; t < 4; ++t) *(v4u*)((char*)lds + slot * 4096 + t * 1024 + lane * 16) = pk[t];
        LGKM0();
        if (lane == 0) atomicAdd(&lds[LDS_CTL / 4 + C_GROUP + (slot >> 2)], 1u);
        FENCE();
    }
    __device__ __forceinline__ void finish(unsigned* lds, int slot, int lane) {
        packq(4); packq(5); packq(6); packq(7);
        stash(lds, slot, lane);
#pragma unroll
        for (int t = 0; t < 4; ++t) {
            auto r = __builtin_amdgcn_permlane32_swap(cs[t], cs[t], false, false);
            cs[t] = r[0] + r[1];
            r = __builtin_amdgcn_permlane16_swap(cs[t], cs[t], false, false);
            cs[t] = r[0] + r[1];
        }
        const int lo = (lane & 16) ? cs[1] : cs[0], hi = (lane & 16) ? cs[3] : cs[2];
        atomicAdd(&lds[LDS_CS / 4 + 4 * (lane & 15) + (lane >> 4)], (unsigned)((lane & 32) ? hi : lo));
        FENCE();
        if (lane == 0) atomicAdd(&lds[LDS_CTL / 4 + C_CSUM], 1u);
    }
};

__global__ __launch_bounds__(NT) __attribute__((aligned(4096)))
void w8a8_kernel(const int* __restrict__ qx, const int* __restrict__ qw,
                 const unsigned short* __restrict__ sxp, const int* __restrict__ zxp,
                 const unsigned short* __restrict__ swp, const unsigned short* __restrict__ biasp,
                 unsigned short* __restrict__ out, char* __restrict__ ws) {
    __shared__ __attribute__((aligned(16))) unsigned lds[LDS_BYTES / 4];
    const int wv = __builtin_amdgcn_readfirstlane(threadIdx.x >> 6);
    const bool conv = blockIdx.z == 0;
    char* ldsb = (char*)lds;
    lds_vu32* ctl = (lds_vu32*)(lds + LDS_CTL / 4);
#define IDS() \
    int tid_ = threadIdx.x, grp = blockIdx.x, mt = blockIdx.y, bz = blockIdx.z; \
    asm volatile("" : "+v"(tid_)); \
    asm volatile("" : "+s"(grp), "+s"(mt), "+s"(bz)); \
    const int lane = tid_ & 63, g = lane >> 4, c = lane & 15; \
    const int bid = (bz * 4 + mt) * 8 + grp, n0 = (grp * 7 + bz - 1) * BN; \
    auto kstep_off = [&](int slot) { const int s = (4 * mt + slot) & 15; return (unsigned)((128 * (s >> 1) + 16 * (s & 1)) * (N_ * 4) + n0 * 4); }; \
    (void)g; (void)c; (void)bid; (void)kstep_off

    {
        if (__builtin_expect(wv < (conv ? 8 : 4), 1)) {
            IDS();
            Loader L;
            unsigned voff, xh = 0;
            if (conv) {
                voff = (lane >> 3) * (K_ * 4) + (lane & 7) * 16;
                unsigned so = (mt * 8 + grp) * (16 * K_ * 4) + wv * 512;
                asm volatile("" : "+s"(so));
                asm volatile("" : "+v"(voff));
                PIN_ARGS();
                auto rx = __builtin_amdgcn_make_buffer_rsrc((void*)qx, 0, M_ * K_ * 4, 0x00020000);
#pragma unroll
                for (int n = 0; n < 8; ++n) {
                    L.wr[n >> 2][n & 3] = __builtin_amdgcn_raw_buffer_load_b128(rx, voff + (n & 3) * 128, so + (n >> 2) * (8 * K_ * 4), 0);
                    FENCE();
                }
            } else {
                voff = g * (4 * N_ * 4) + c * 16;
                unsigned so = (256 * mt + 128 * (wv >> 1) + 16 * (wv & 1)) * (N_ * 4) + n0 * 4;
                asm volatile("" : "+s"(so));
                asm volatile("" : "+v"(voff));
                PIN_ARGS();
                auto rw = __builtin_amdgcn_make_buffer_rsrc((void*)qw, 0, K_ * N_ * 4, 0x00020000);
#pragma unroll
                for (int n = 0; n < 8; ++n) {
                    L.wr[n >> 2][n & 3] = __builtin_amdgcn_raw_buffer_load_b128(rw, voff, so + (32 * (n >> 2) + (n & 3)) * (N_ * 4), 0);
                    FENCE();
                }
                if (wv == 0) {
                    auto rx = __builtin_amdgcn_make_buffer_rsrc((void*)qx, 0, M_ * K_ * 4, 0x00020000);
                    unsigned hl = (bz - 1) * 37 + (lane < 36 ? lane : 36);
                    hl = hl < 255u ? hl : 255u;
                    xh = __builtin_amdgcn_raw_buffer_load_b32(rx, ((hl & 0xf0) << 8) | ((hl & 15) << 7), (mt * 8 + grp) * (16 * K_ * 4) + 2048, 0);
                    FENCE();
                }
            }
            asm volatile("" ::: "memory");
            if (conv) {
                const int r8 = lane >> 3, q8 = lane & 7;
                auto rx8 = __builtin_amdgcn_make_buffer_rsrc((void*)(ws + WS_X8), 0, M_ * K_, 0x00020000);
                char* xt = ldsb + wv * 2048;
#pragma unroll
                for (int hh = 0; hh < 2; ++hh) {
                    v4u p;
#pragma unroll
                    for (int i = 0; i < 4; ++i) { const v4u v = L.wr[hh][i]; p[i] = pack4(v[0], v[1], v[2], v[3]); }
                    *(v4u*)(xt + q8 * 256 + (((8 * hh + r8) ^ q8) * 16)) = p;
                }
#pragma unroll
                for (int s = 0; s < 2; ++s) {
                    const int q = 4 * s + g;
                    const v4u a = *(const v4u*)(xt + q * 256 + ((c ^ q) * 16));
                    __builtin_amdgcn_raw_buffer_store_b128(a, rx8, ((bid * 16 + 2 * wv + s) * 64 + lane) * 16, 0, SC1);
                }
                VMWAIT(0);
                BAR();
                return;
            }
            BAR();
            auto rw = __builtin_amdgcn_make_buffer_rsrc((void*)qw, 0, K_ * N_ * 4, 0x00020000);
            const unsigned soA = kstep_off(wv);
#pragma unroll
            for (int n = 8; n < 16; ++n) {
                L.wr[n >> 2][n & 3] = __builtin_amdgcn_raw_buffer_load_b128(rw, voff, soA + (32 * (n >> 2) + (n & 3)) * (N_ * 4), 0);
                FENCE();
            }
            L.cs[0] = L.cs[1] = L.cs[2] = L.cs[3] = 0;
            L.packq(0);
            VMWAIT(8);
            if (lane == 0) atomicAdd(&lds[LDS_CTL / 4 + C_OWN], 1u);
            L.packq(1); L.packq(2); L.packq(3);
            if (lane == 0) atomicAdd(&lds[LDS_CTL / 4 + C_OWN], 1u);
            L.stash(lds, wv, lane);
            asm volatile("" :: "v"(xh));
            const unsigned soB = kstep_off(wv + 4);
#pragma unroll
            for (int n = 0; n < 16; ++n) {
                L.wr[4 + (n >> 2)][n & 3] = __builtin_amdgcn_raw_buffer_load_b128(rw, voff, soB + (32 * (n >> 2) + (n & 3)) * (N_ * 4), 0);
                FENCE();
            }
            L.finish(lds, wv + 4, lane);
            return;
        }
        PIN_ARGS();
        if (conv) {
            if (wv != 8) return;
            IDS();
            const u64 tag = ld_agent(ws) + TAG_STEP;
            BAR();
            if (lane == 0) { st_agent(ws + WS_FLAG + bid * FLAG_STRIDE, tag); st_agent(ws + WS_ACK + bid * 8, tag); }
            if (bid == 0) {
                __builtin_amdgcn_s_sleep(60);
#pragma nounroll
                for (unsigned spin = 0; spin < SPIN_CAP; ++spin) {
                    bool ok = true;
#pragma unroll
                    for (int k = 0; k < 4; ++k) ok &= (ld_agent(ws + WS_ACK + (k * 64 + lane) * 8) == tag);
                    if (__all(ok)) break;
                    __builtin_amdgcn_s_sleep(8);
                }
                if (lane == 0) st_agent(ws, tag);
            }
            return;
        }
        if (wv < 8) {
            BAR();
            IDS();
            auto rw = __builtin_amdgcn_make_buffer_rsrc((void*)qw, 0, K_ * N_ * 4, 0x00020000);
            const unsigned soA = kstep_off(wv + 4), soB = kstep_off(wv + 8);
            const unsigned voff = g * (4 * N_ * 4) + c * 16;
            Loader L;
            L.cs[0] = L.cs[1] = L.cs[2] = L.cs[3] = 0;
            auto ld1 = [&](int n) {
                L.wr[n >> 2][n & 3] = __builtin_amdgcn_raw_buffer_load_b128(rw, voff, (n < 16 ? soA : soB) + (32 * ((n >> 2) & 3) + (n & 3)) * (N_ * 4), 0);
                FENCE();
            };
            lds_wait_ge(ctl + C_OWN, 4);
#pragma unroll
            for (int n = 0; n < 16; ++n) ld1(n);
#pragma unroll
            for (int n = 0; n < 16; ++n) {
                VMWAIT(15);
                ld1(n + 16);
                if ((n & 3) == 3) L.packq(n >> 2);
            }
            L.stash(lds, wv + 4, lane);
            L.finish(lds, wv + 8, lane);
            return;
        }
    }

    IDS();
    const int mw = wv - 8;
    u64 tag;
    if (mw == 0) {
        tag = ld_agent(ws);
        const unsigned par = *(const unsigned*)((lane < 32 ? swp : biasp) + n0 + 2 * (lane & 31));
        const unsigned psx = __builtin_nontemporal_load(sxp);
        const unsigned pzx = __builtin_nontemporal_load(zxp);
        FENCE();
        lds[LDS_CTL / 4 + lane] = 0;
        lds[LDS_CS / 4 + lane] = 0;
        BAR();
        tag += TAG_STEP;
        lds[LDS_PAR / 4 + lane] = par;
        lds[LDS_PAR / 4 + 64] = psx;
        lds[LDS_PAR / 4 + 65] = pzx;
        FENCE();
        ctl[C_TAG_LO] = (unsigned)tag; ctl[C_TAG_HI] = (unsigned)(tag >> 32); ctl[C_PAR] = 1;
        if (lane == 0) st_agent(ws + WS_ACK + bid * 8, tag);
    } else {
        BAR();
        lds_wait_ge(ctl + C_PAR, 1);
        tag = ((u64)ctl[C_TAG_HI] << 32) | ctl[C_TAG_LO];
    }
    auto rx8 = __builtin_amdgcn_make_buffer_rsrc((void*)(ws + WS_X8), 0, M_ * K_, 0x00020000);
    const unsigned x8off = (mt * 8 + 2 * mw) * 16384 + lane * 16;
    v4i xa[16][2], bf[16][4], acc[2][4];
    auto ldx = [&](int u, int m) {
        xa[u][m] = __builtin_bit_cast(v4i, __builtin_amdgcn_raw_buffer_load_b128(rx8, x8off, ((4 * mt + u) & 15) * 1024 + m * 16384, SC1));
        FENCE();
    };
    auto ldb = [&](int u, int t) { bf[u][t] = *(const v4i*)(ldsb + u * 4096 + t * 1024 + lane * 16); FENCE(); };
#pragma unroll
    for (int t = 0; t < 8; ++t) acc[t >> 2][t & 3] = (v4i){0, 0, 0, 0};
    v4f scv, ofv;
    float zf;
    {
        const v2u swv = *(const v2u*)(ldsb + LDS_PAR + 8 * c), biv = *(const v2u*)(ldsb + LDS_PAR + 128 + 8 * c);
        const v2u szv = *(const v2u*)(ldsb + LDS_PAR + 256);
        const float sx = bf2f((unsigned short)szv[0]);
        zf = rnd_bf16((float)(int)szv[1]);
#pragma unroll
        for (int t = 0; t < 4; ++t) {
            scv[t] = __builtin_bit_cast(float, (t & 1) ? (swv[t >> 1] & 0xffff0000u) : (swv[t >> 1] << 16)) * sx;
            ofv[t] = __builtin_bit_cast(float, (t & 1) ? (biv[t >> 1] & 0xffff0000u) : (biv[t >> 1] << 16));
        }
        asm volatile("" : "+v"(scv), "+v"(ofv), "+v"(zf));
    }
    lds_wait_ge(ctl + C_OWN, 8);
    poll_flags(ws + WS_FLAG + (mt * 8 + 2 * mw + (lane & 1)) * FLAG_STRIDE, tag);
#pragma unroll
    for (int p = 0; p < 2 * XD; ++p) ldx(p >> 1, p & 1);
    v4i csv;
    unsigned rdy = 0;
    lds_wait_ge(ctl + C_GROUP, 4);
#pragma unroll
    for (int t = 0; t < 4; ++t) ldb(0, t);
#pragma unroll
    for (int u = 0; u < 16; ++u) {
#pragma unroll
        for (int k = 0; k < 8; ++k) {
            acc[k >> 2][k & 3] = __builtin_amdgcn_mfma_i32_16x16x64_i8(xa[u][k >> 2], bf[u][k & 3], acc[k >> 2][k & 3], 0, 0, 0);
            FENCE();
            if (k == 0 && u + 1 < 16 && ((u + 1) & 3) == 0) {
                if ((unsigned)__builtin_amdgcn_readfirstlane(rdy) < 4) lds_wait_ge(ctl + C_GROUP + ((u + 1) >> 2), 4);
                FENCE();
            }
            if (k < 4 && u + 1 < 16) ldb(u + 1, k);
            if ((k == 4 || k == 5) && u + XD < 16) ldx(u + XD, k - 4);
            if (k == 6 && u + 2 < 16 && ((u + 2) & 3) == 0) { rdy = ctl[C_GROUP + ((u + 2) >> 2)]; FENCE(); }
            if (k == 6 && u == 13) { rdy = ctl[C_CSUM]; FENCE(); }
            if (k == 4 && u == 14) {
                if ((unsigned)__builtin_amdgcn_readfirstlane(rdy) < 8) lds_wait_ge(ctl + C_CSUM, 8);
                FENCE();
                csv = *(const v4i*)(ldsb + LDS_CS + 16 * c);
                FENCE();
            }
            if (u == 15 && k < 4) {
                ofv[k] -= rnd_bf16(rnd_bf16((float)csv[k]) * zf) * scv[k];
                asm volatile("" : "+v"(ofv));
                FENCE();
            }
        }
    }
    auto ro = __builtin_amdgcn_make_buffer_rsrc((void*)out, 0, M_ * N_ * 2, 0x00020000);
    const unsigned ooff = ((mt * BM + 32 * mw + 4 * g) * N_ + n0 + 4 * c) * 2;
#pragma unroll
    for (int m = 0; m < 2; ++m)
#pragma unroll
        for (int r = 0; r < 4; ++r) {
            v2u o;
#pragma unroll
            for (int p = 0; p < 2; ++p) {
                const v2f v = (v2f){(float)acc[m][2 * p][r], (float)acc[m][2 * p + 1][r]} * (v2f){scv[2 * p], scv[2 * p + 1]}
                              + (v2f){ofv[2 * p], ofv[2 * p + 1]};
                o[p] = __builtin_bit_cast(unsigned, __builtin_convertvector(v, v2bf));
            }
            __builtin_amdgcn_raw_buffer_store_b64(o, ro, ooff, (16 * m + r) * N_ * 2, SC1);
        }
}

extern "C" void kernel_launch(void* const* d_in, const int* in_sizes, int n_in,
                              void* d_out, int out_size, void* d_ws, size_t ws_size,
                              hipStream_t stream) {
    w8a8_kernel<<<dim3(8, 4, 8), NT, 0, stream>>>((const int*)d_in[0], (const int*)d_in[1],
                                         (const unsigned short*)d_in[2], (const int*)d_in[3],
                                         (const unsigned short*)d_in[4], (const unsigned short*)d_in[5],
                                         (unsigned short*)d_out, (char*)d_ws);
}
```

```cpp
#include <hip/hip_runtime.h>
#include <hip/hip_bf16.h>
#include <stdint.h>

typedef int v4i __attribute__((ext_vector_type(4)));
typedef unsigned v4u __attribute__((ext_vector_type(4)));
typedef unsigned v2u __attribute__((ext_vector_type(2)));
typedef float v2f __attribute__((ext_vector_type(2)));
typedef float v4f __attribute__((ext_vector_type(4)));
typedef __bf16 v2bf __attribute__((ext_vector_type(2)));
typedef unsigned long long u64;
typedef __attribute__((address_space(1))) u64 gu64;
typedef __attribute__((address_space(3))) volatile unsigned lds_vu32;

constexpr int M_ = 512, K_ = 1024, N_ = 3584;
constexpr int BM = 128, BN = 64, NT = 12 * 64;
constexpr unsigned FLAG_STRIDE = 256, WS_FLAG = 4096, WS_ACK = 32768, WS_X8 = 262144;
constexpr u64 TAG_STEP = 0x9E3779B97F4A7C15ull;
constexpr int SC1 = 16;
constexpr unsigned SPIN_CAP = 1u << 15;
constexpr int LDS_CTL = 65536, LDS_CS = LDS_CTL + 256, LDS_PAR = LDS_CS + 256;
constexpr int LDS_BYTES = 84 * 1024;
enum { C_TAG_LO = 0, C_TAG_HI = 1, C_PAR = 2, C_OWN = 4, C_GROUP = 8, C_CSUM = 12 };
constexpr int XD = 4;

#define PIN_ARGS() asm volatile("" :: "s"(qx), "s"(qw), "s"(sxp), "s"(zxp), "s"(swp), "s"(biasp), "s"(out), "s"(ws))
#define FENCE() do { asm volatile("" ::: "memory"); __builtin_amdgcn_sched_barrier(0); } while (0)
#define VMWAIT(n) do { asm volatile("s_waitcnt vmcnt(%0)" :: "i"(n) : "memory"); __builtin_amdgcn_sched_barrier(0); } while (0)
#define LGKM0() do { asm volatile("s_waitcnt lgkmcnt(0)" ::: "memory"); __builtin_amdgcn_sched_barrier(0); } while (0)
#define BAR() do { asm volatile("s_waitcnt lgkmcnt(0)\n\ts_barrier" ::: "memory"); __builtin_amdgcn_sched_barrier(0); } while (0)

__device__ __forceinline__ unsigned pack4(unsigned a, unsigned b, unsigned c, unsigned d) {
    return __builtin_amdgcn_perm(b, a, 0x0c0c0400u) | __builtin_amdgcn_perm(d, c, 0x04000c0cu);
}
__device__ __forceinline__ float rnd_bf16(float v) {
    const v2bf b = __builtin_convertvector((v2f){v, 0.f}, v2bf);
    return __builtin_bit_cast(float, __builtin_bit_cast(unsigned, b) << 16);
}
__device__ __forceinline__ float bf2f(unsigned short h) { return __builtin_bit_cast(float, (unsigned)h << 16); }
__device__ __forceinline__ u64 ld_agent(const void* p) { return __hip_atomic_load((gu64*)p, __ATOMIC_RELAXED, __HIP_MEMORY_SCOPE_AGENT); }
__device__ __forceinline__ void st_agent(void* p, u64 v) { __hip_atomic_store((gu64*)p, v, __ATOMIC_RELAXED, __HIP_MEMORY_SCOPE_AGENT); }
__device__ __forceinline__ void lds_wait_ge(lds_vu32* w, unsigned v) {
#pragma nounroll
    for (unsigned spin = 0; spin < (SPIN_CAP << 4); ++spin) {
        if ((unsigned)__builtin_amdgcn_readfirstlane(*w) >= v) break;
        __builtin_amdgcn_s_sleep(1);
    }
    FENCE();
}
__device__ __forceinline__ void poll_flags(const char* p, u64 tag) {
#pragma nounroll
    for (unsigned spin = 0; spin < SPIN_CAP; ++spin) {
        if (__all(ld_agent(p) == tag)) break;
        __builtin_amdgcn_s_sleep(8);
    }
    FENCE();
}

struct Loader {
    v4u wr[8][4], pk[4];
    int cs[4];
    __device__ __forceinline__ void packq(int i) {
#pragma unroll
        for (int t = 0; t < 4; ++t) {
            unsigned p = pack4(wr[i][0][t], wr[i][1][t], wr[i][2][t], wr[i][3][t]);
            cs[t] = __builtin_amdgcn_sdot4(p, 0x01010101, cs[t], false);
            asm volatile("" : "+v"(p), "+v"(cs[t]));
            pk[t][i & 3] = p;
        }
        FENCE();
    }
    __device__ __forceinline__ void stash(unsigned* lds, int slot, int lane) {
#pragma unroll
        for (int t = 0; t < 4; ++t) *(v4u*)((char*)lds + slot * 4096 + t * 1024 + lane * 16) = pk[t];
        LGKM0();
        if (lane == 0) atomicAdd(&lds[LDS_CTL / 4 + C_GROUP + (slot >> 2)], 1u);
        FENCE();
    }
    __device__ __forceinline__ void finish(unsigned* lds, int slot, int lane) {
        packq(4); packq(5); packq(6); packq(7);
        stash(lds, slot, lane);
#pragma unroll
        for (int t = 0; t < 4; ++t) {
            auto r = __builtin_amdgcn_permlane32_swap(cs[t], cs[t], false, false);
            cs[t] = r[0] + r[1];
            r = __builtin_amdgcn_permlane16_swap(cs[t], cs[t], false, false);
            cs[t] = r[0] + r[1];
        }
        const int lo = (lane & 16) ? cs[1] : cs[0], hi = (lane & 16) ? cs[3] : cs[2];
        atomicAdd(&lds[LDS_CS / 4 + 4 * (lane & 15) + (lane >> 4)], (unsigned)((lane & 32) ? hi : lo));
        FENCE();
        if (lane == 0) atomicAdd(&lds[LDS_CTL / 4 + C_CSUM], 1u);
    }
};

__global__ __launch_bounds__(NT) __attribute__((aligned(4096)))
void w8a8_kernel(const int* __restrict__ qx, const int* __restrict__ qw,
                 const unsigned short* __restrict__ sxp, const int* __restrict__ zxp,
                 const unsigned short* __restrict__ swp, const unsigned short* __restrict__ biasp,
                 unsigned short* __restrict__ out, char* __restrict__ ws) {
    __shared__ __attribute__((aligned(16))) unsigned lds[LDS_BYTES / 4];
    const int wv = __builtin_amdgcn_readfirstlane(threadIdx.x >> 6);
    const bool conv = blockIdx.z == 0;
    char* ldsb = (char*)lds;
    lds_vu32* ctl = (lds_vu32*)(lds + LDS_CTL / 4);
#define IDS() \
    int tid_ = threadIdx.x, grp = blockIdx.x, mt = blockIdx.y, bz = blockIdx.z; \
    asm volatile("" : "+v"(tid_)); \
    asm volatile("" : "+s"(grp), "+s"(mt), "+s"(bz)); \
    const int lane = tid_ & 63, g = lane >> 4, c = lane & 15; \
    const int bid = (bz * 4 + mt) * 8 + grp, n0 = (grp * 7 + bz - 1) * BN; \
    auto kstep_off = [&](int slot) { const int s = (4 * mt + slot) & 15; return (unsigned)((128 * (s >> 1) + 16 * (s & 1)) * (N_ * 4) + n0 * 4); }; \
    (void)g; (void)c; (void)bid; (void)kstep_off

    {
        if (__builtin_expect(wv < (conv ? 8 : 4), 1)) {
            IDS();
            Loader L;
            unsigned voff, xh = 0;
            if (conv) {
                voff = (lane >> 3) * (K_ * 4) + (lane & 7) * 16;
                unsigned so = (mt * 8 + grp) * (16 * K_ * 4) + wv * 512;
                asm volatile("" : "+s"(so));
                asm volatile("" : "+v"(voff));
                PIN_ARGS();
                auto rx = __builtin_amdgcn_make_buffer_rsrc((void*)qx, 0, M_ * K_ * 4, 0x00020000);
#pragma unroll
                for (int n = 0; n < 8; ++n) {
                    L.wr[n >> 2][n & 3] = __builtin_amdgcn_raw_buffer_load_b128(rx, voff + (n & 3) * 128, so + (n >> 2) * (8 * K_ * 4), 0);
                    FENCE();
                }
            } else {
                voff = g * (4 * N_ * 4) + c * 16;
                unsigned so = (256 * mt + 128 * (wv >> 1) + 16 * (wv & 1)) * (N_ * 4) + n0 * 4;
                asm volatile("" : "+s"(so));
                asm volatile("" : "+v"(voff));
                PIN_ARGS();
                auto rw = __builtin_amdgcn_make_buffer_rsrc((void*)qw, 0, K_ * N_ * 4, 0x00020000);
#pragma unroll
                for (int n = 0; n < 8; ++n) {
                    L.wr[n >> 2][n & 3] = __builtin_amdgcn_raw_buffer_load_b128(rw, voff, so + (32 * (n >> 2) + (n & 3)) * (N_ * 4), 0);
                    FENCE();
                }
                if (wv == 0) {
                    auto rx = __builtin_amdgcn_make_buffer_rsrc((void*)qx, 0, M_ * K_ * 4, 0x00020000);
                    unsigned hl = (bz - 1) * 37 + (lane < 36 ? lane : 36);
                    hl = hl < 255u ? hl : 255u;
                    xh = __builtin_amdgcn_raw_buffer_load_b32(rx, ((hl & 0xf0) << 8) | ((hl & 15) << 7), (mt * 8 + grp) * (16 * K_ * 4) + 2048, 0);
                    FENCE();
                }
            }
            asm volatile("" ::: "memory");
            if (conv) {
                const int r8 = lane >> 3, q8 = lane & 7;
                auto rx8 = __builtin_amdgcn_make_buffer_rsrc((void*)(ws + WS_X8), 0, M_ * K_, 0x00020000);
                char* xt = ldsb + wv * 2048;
#pragma unroll
                for (int hh = 0; hh < 2; ++hh) {
                    v4u p;
#pragma unroll
                    for (int i = 0; i < 4; ++i) { const v4u v = L.wr[hh][i]; p[i] = pack4(v[0], v[1], v[2], v[3]); }
                    *(v4u*)(xt + q8 * 256 + (((8 * hh + r8) ^ q8) * 16)) = p;
                }
#pragma unroll
                for (int s = 0; s < 2; ++s) {
                    const int q = 4 * s + g;
                    const v4u a = *(const v4u*)(xt + q * 256 + ((c ^ q) * 16));
                    __builtin_amdgcn_raw_buffer_store_b128(a, rx8, ((bid * 16 + 2 * wv + s) * 64 + lane) * 16, 0, SC1);
                }
                VMWAIT(0);
                BAR();
                return;
            }
            BAR();
            auto rw = __builtin_amdgcn_make_buffer_rsrc((void*)qw, 0, K_ * N_ * 4, 0x00020000);
            const unsigned soA = kstep_off(wv);
#pragma unroll
            for (int n = 8; n < 16; ++n) {
                L.wr[n >> 2][n & 3] = __builtin_amdgcn_raw_buffer_load_b128(rw, voff, soA + (32 * (n >> 2) + (n & 3)) * (N_ * 4), 0);
                FENCE();
            }
            L.cs[0] = L.cs[1] = L.cs[2] = L.cs[3] = 0;
            L.packq(0);
            VMWAIT(8);
            if (lane == 0) atomicAdd(&lds[LDS_CTL / 4 + C_OWN], 1u);
            L.packq(1); L.packq(2); L.packq(3);
            if (lane == 0) atomicAdd(&lds[LDS_CTL / 4 + C_OWN], 1u);
            L.stash(lds, wv, lane);
            asm volatile("" :: "v"(xh));
            const unsigned soB = kstep_off(wv + 4);
#pragma unroll
            for (int n = 0; n < 16; ++n) {
                L.wr[4 + (n >> 2)][n & 3] = __builtin_amdgcn_raw_buffer_load_b128(rw, voff, soB + (32 * (n >> 2) + (n & 3)) * (N_ * 4), 0);
                FENCE();
            }
            L.finish(lds, wv + 4, lane);
            return;
        }
        PIN_ARGS();
        if (conv) {
            if (wv != 8) return;
            IDS();
            const u64 tag = ld_agent(ws) + TAG_STEP;
            BAR();
            if (lane == 0) { st_agent(ws + WS_FLAG + bid * FLAG_STRIDE, tag); st_agent(ws + WS_ACK + bid * 8, tag); }
            if (bid == 0) {
                __builtin_amdgcn_s_sleep(60);
#pragma nounroll
                for (unsigned spin = 0; spin < SPIN_CAP; ++spin) {
                    bool ok = true;
#pragma unroll
                    for (int k = 0; k < 4; ++k) ok &= (ld_agent(ws + WS_ACK + (k * 64 + lane) * 8) == tag);
                    if (__all(ok)) break;
                    __builtin_amdgcn_s_sleep(8);
                }
                if (lane == 0) st_agent(ws, tag);
            }
            return;
        }
        if (wv < 8) {
            BAR();
            IDS();
            auto rw = __builtin_amdgcn_make_buffer_rsrc((void*)qw, 0, K_ * N_ * 4, 0x00020000);
            const unsigned soA = kstep_off(wv + 4), soB = kstep_off(wv + 8);
            const unsigned voff = g * (4 * N_ * 4) + c * 16;
            Loader L;
            L.cs[0] = L.cs[1] = L.cs[2] = L.cs[3] = 0;
            auto ld1 = [&](int n) {
                L.wr[n >> 2][n & 3] = __builtin_amdgcn_raw_buffer_load_b128(rw, voff, (n < 16 ? soA : soB) + (32 * ((n >> 2) & 3) + (n & 3)) * (N_ * 4), 0);
                FENCE();
            };
            lds_wait_ge(ctl + C_OWN, 4);
#pragma unroll
            for (int n = 0; n < 16; ++n) ld1(n);
#pragma unroll
            for (int n = 0; n < 16; ++n) {
                VMWAIT(15);
                ld1(n + 16);
                if ((n & 3) == 3) L.packq(n >> 2);
            }
            L.stash(lds, wv + 4, lane);
            L.finish(lds, wv + 8, lane);
            return;
        }
    }

    IDS();
    const int mw = wv - 8;
    u64 tag;
    if (mw == 0) {
        tag = ld_agent(ws);
        const unsigned par = *(const unsigned*)((lane < 32 ? swp : biasp) + n0 + 2 * (lane & 31));
        const unsigned psx = __builtin_nontemporal_load(sxp);
        const unsigned pzx = __builtin_nontemporal_load(zxp);
        FENCE();
        lds[LDS_CTL / 4 + lane] = 0;
        lds[LDS_CS / 4 + lane] = 0;
        BAR();
        tag += TAG_STEP;
        lds[LDS_PAR / 4 + lane] = par;
        lds[LDS_PAR / 4 + 64] = psx;
        lds[LDS_PAR / 4 + 65] = pzx;
        FENCE();
        ctl[C_TAG_LO] = (unsigned)tag; ctl[C_TAG_HI] = (unsigned)(tag >> 32); ctl[C_PAR] = 1;
        if (lane == 0) st_agent(ws + WS_ACK + bid * 8, tag);
    } else {
        BAR();
        lds_wait_ge(ctl + C_PAR, 1);
        tag = ((u64)ctl[C_TAG_HI] << 32) | ctl[C_TAG_LO];
    }
    auto rx8 = __builtin_amdgcn_make_buffer_rsrc((void*)(ws + WS_X8), 0, M_ * K_, 0x00020000);
    const unsigned x8off = (mt * 8 + 2 * mw) * 16384 + lane * 16;
    v4i xa[16][2], bf[16][4], acc[2][4];
    auto ldx = [&](int u, int m) {
        xa[u][m] = __builtin_bit_cast(v4i, __builtin_amdgcn_raw_buffer_load_b128(rx8, x8off, ((4 * mt + u) & 15) * 1024 + m * 16384, SC1));
        FENCE();
    };
    auto ldb = [&](int u, int t) { bf[u][t] = *(const v4i*)(ldsb + u * 4096 + t * 1024 + lane * 16); FENCE(); };
#pragma unroll
    for (int t = 0; t < 8; ++t) acc[t >> 2][t & 3] = (v4i){0, 0, 0, 0};
    v4f scv, ofv;
    float zf;
    {
        const v2u swv = *(const v2u*)(ldsb + LDS_PAR + 8 * c), biv = *(const v2u*)(ldsb + LDS_PAR + 128 + 8 * c);
        const v2u szv = *(const v2u*)(ldsb + LDS_PAR + 256);
        const float sx = bf2f((unsigned short)szv[0]);
        zf = rnd_bf16((float)(int)szv[1]);
#pragma unroll
        for (int t = 0; t < 4; ++t) {
            scv[t] = __builtin_bit_cast(float, (t & 1) ? (swv[t >> 1] & 0xffff0000u) : (swv[t >> 1] << 16)) * sx;
            ofv[t] = __builtin_bit_cast(float, (t & 1) ? (biv[t >> 1] & 0xffff0000u) : (biv[t >> 1] << 16));
        }
        asm volatile("" : "+v"(scv), "+v"(ofv), "+v"(zf));
    }
    unsigned ooff = ((mt * BM + 32 * mw + 4 * g) * N_ + n0 + 4 * c) * 2;
    asm volatile("" : "+v"(ooff));
    lds_wait_ge(ctl + C_OWN, 8);
    poll_flags(ws + WS_FLAG + (mt * 8 + 2 * mw + (lane & 1)) * FLAG_STRIDE, tag);
#pragma unroll
    for (int p = 0; p < 2 * XD; ++p) ldx(p >> 1, p & 1);
    v4i csv;
    v2f ev[2] = {{0.f, 0.f}, {0.f, 0.f}};
    unsigned eu[2] = {0u, 0u};
    unsigned rdy = 0;
    lds_wait_ge(ctl + C_GROUP, 4);
#pragma unroll
    for (int t = 0; t < 4; ++t) ldb(0, t);
#pragma unroll
    for (int u = 0; u < 16; ++u) {
#pragma unroll
        for (int k = 0; k < 8; ++k) {
            acc[k >> 2][k & 3] = __builtin_amdgcn_mfma_i32_16x16x64_i8(xa[u][k >> 2], bf[u][k & 3], acc[k >> 2][k & 3], 0, 0, 0);
            FENCE();
            if (k == 0 && u + 1 < 16 && ((u + 1) & 3) == 0) {
                if ((unsigned)__builtin_amdgcn_readfirstlane(rdy) < 4) lds_wait_ge(ctl + C_GROUP + ((u + 1) >> 2), 4);
                FENCE();
            }
            if (k < 4 && u + 1 < 16) ldb(u + 1, k);
            if ((k == 4 || k == 5) && u + XD < 16) ldx(u + XD, k - 4);
            if (k == 6 && u + 2 < 16 && ((u + 2) & 3) == 0) { rdy = ctl[C_GROUP + ((u + 2) >> 2)]; FENCE(); }
            if (k == 6 && u == 13) { rdy = ctl[C_CSUM]; FENCE(); }
            if (k == 3 && u == 14) {
                if ((unsigned)__builtin_amdgcn_readfirstlane(rdy) < 8) lds_wait_ge(ctl + C_CSUM, 8);
                FENCE();
                csv = *(const v4i*)(ldsb + LDS_CS + 16 * c);
                FENCE();
            }
            if (u * 8 + k >= 14 * 8 + 7 && u * 8 + k < 14 * 8 + 7 + 7) {
                const int ph = u * 8 + k - (14 * 8 + 7);
#pragma unroll
                for (int p = 0; p < 2; ++p) {
                    if (ph == 0) ev[p].x = (float)csv[2 * p];
                    if (ph == 1) ev[p].y = (float)csv[2 * p + 1];
                    if (ph == 2 || ph == 5) eu[p] = __builtin_bit_cast(unsigned, __builtin_convertvector(ph == 2 ? ev[p] : ev[p] * zf, v2bf));
                    if (ph == 3 || ph == 6) ev[p].x = __builtin_bit_cast(float, eu[p] << 16);
                    if (ph == 4 || ph == 6) ev[p].y = __builtin_bit_cast(float, eu[p] & 0xffff0000u);
                    asm volatile("" : "+v"(ev[p]), "+v"(eu[p]));
                }
                FENCE();
            }
        }
    }
#pragma unroll
    for (int t = 0; t < 4; ++t) ofv[t] -= ev[t >> 1][t & 1] * scv[t];
    auto ro = __builtin_amdgcn_make_buffer_rsrc((void*)out, 0, M_ * N_ * 2, 0x00020000);
#pragma unroll
    for (int m = 0; m < 2; ++m)
#pragma unroll
        for (int r = 0; r < 4; ++r) {
            v2u o;
#pragma unroll
            for (int p = 0; p < 2; ++p) {
                const v2f v = (v2f){(float)acc[m][2 * p][r], (float)acc[m][2 * p + 1][r]} * (v2f){scv[2 * p], scv[2 * p + 1]}
                              + (v2f){ofv[2 * p], ofv[2 * p + 1]};
                o[p] = __builtin_bit_cast(unsigned, __builtin_convertvector(v, v2bf));
            }
            __builtin_amdgcn_raw_buffer_store_b64(o, ro, ooff, (16 * m + r) * N_ * 2, SC1);
        }
}

extern "C" void kernel_launch(void* const* d_in, const int* in_sizes, int n_in,
                              void* d_out, int out_size, void* d_ws, size_t ws_size,
                              hipStream_t stream) {
    w8a8_kernel<<<dim3(8, 4, 8), NT, 0, stream>>>((const int*)d_in[0], (const int*)d_in[1],
                                         (const unsigned short*)d_in[2], (const int*)d_in[3],
                                         (const unsigned short*)d_in[4], (const unsigned short*)d_in[5],
                                         (unsigned short*)d_out, (char*)d_ws);
}
```
